```python
import torch

def _softmax(x, axis=-1):
    return torch.softmax(x, dim=axis)
def _gelu(x):
    return torch.nn.functional.gelu(x, approximate="tanh")
def _silu(x):
    return torch.nn.functional.silu(x)
def _sigmoid(x):
    return torch.sigmoid(x)
def _layernorm(x, axis=-1, eps=1e-5):
    m = x.mean(dim=axis, keepdim=True)
    v = x.var(dim=axis, keepdim=True, unbiased=False)
    return (x - m) / torch.sqrt(v + eps)
def _scatter_add(tgt, idx, src, axis=0):
    return tgt.clone().index_add_(axis, idx, src)
def _segment_sum(src, seg, n):
    out = torch.zeros((n,) + src.shape[1:], dtype=src.dtype)
    return out.index_add_(0, seg, src)
def _pad(x, pw):
    flat = tuple(v for pair in reversed(pw) for v in pair)
    return torch.nn.functional.pad(x, flat)

kernel_name = 'parallel_sum__mha_block_cached__glu_gated_glu__decode'

def setup_inputs():

    _w = (lambda a: (lambda t: t.to(torch.bfloat16) if t.is_floating_point() else t)(torch.as_tensor(a)))
    def _A_setup():
        B, Sq, D = 1, 1, 640
        H, Sk, Dh = 32, 4096, 192
        return {
            "x": _w((lambda *s: torch.randn(*s, dtype=torch.bfloat16) * 0.05)(B, Sq, D)),
            "k_cache": _w((lambda *s: torch.randn(*s, dtype=torch.bfloat16) * 3.2)(B, H, Sk, Dh)),
            "v_cache": _w((lambda *s: torch.randn(*s, dtype=torch.bfloat16) * 1.6)(B, H, Sk, Dh)),
            "wq": _w((lambda *s: torch.randn(*s, dtype=torch.bfloat16) * 3.2)(D, H * Dh) * (D ** -0.5)),
            "wo": _w((lambda *s: torch.randn(*s, dtype=torch.bfloat16) * 1.6)(H * Dh, D) * ((H * Dh) ** -0.5)),
        }
    _A_in = _A_setup()

    def _B_setup():
        B, S, D, F = 1, 1, 640, 1536
        return {
            "x": _w((lambda *s: torch.randn(*s, dtype=torch.bfloat16) * 0.05)(B, S, D)),
            "w_gate": _w((lambda *s: torch.randn(*s, dtype=torch.bfloat16) * 1.6)(D, F)),
            "w_up": _w((lambda *s: torch.randn(*s, dtype=torch.bfloat16) * 0.1)(D, F)),
            "w_down": _w((lambda *s: torch.randn(*s, dtype=torch.bfloat16) * 0.1)(F, D)),
        }
    _B_in = _B_setup()

    return {
        "A_x": _A_in["x"],
        "A_k_cache": _A_in["k_cache"],
        "A_v_cache": _A_in["v_cache"],
        "A_wq": _A_in["wq"],
        "A_wo": _A_in["wo"],
        "B_x": _B_in["x"],
        "B_w_gate": _B_in["w_gate"],
        "B_w_up": _B_in["w_up"],
        "B_w_down": _B_in["w_down"],
    }

def reference(A_x, A_k_cache, A_v_cache, A_wq, A_wo, B_x, B_w_gate, B_w_up, B_w_down):
    def _A_ref(x, k_cache, v_cache, wq, wo):
        B, Sq, D = x.shape
        H, Dh = 32, 192
        q = (lambda x, *d: x.permute(*d))((x @ wq).reshape(B, Sq, H, Dh), 0, 2, 1, 3)
        scores = torch.einsum("bhqd,bhkd->bhqk", q, k_cache) * (Dh ** -0.5)
        p = _softmax(scores, axis=-1)
        o = torch.einsum("bhqk,bhkd->bhqd", p, v_cache)
        return (lambda x, *d: x.permute(*d))(o, 0, 2, 1, 3).reshape(B, Sq, H * Dh) @ wo

    def _B_ref(x, w_gate, w_up, w_down):
        g = torch.einsum("bsd,df->bsf", x, w_gate)
        u = torch.einsum("bsd,df->bsf", x, w_up)
        h = _sigmoid(g) * u
        return torch.einsum("bsf,fd->bsd", h, w_down)

    x = A_x
    xn = _layernorm(x, axis=-1)
    return x + _A_ref(xn, A_k_cache, A_v_cache, A_wq, A_wo) + _B_ref(xn, B_w_gate, B_w_up, B_w_down)
```

```cpp
#include <hip/hip_runtime.h>
#include <hip/hip_bf16.h>
#include <stdint.h>

#define MIMIC 0

typedef unsigned short u16;
typedef unsigned v4u __attribute__((ext_vector_type(4)));
typedef __bf16 bf16x2 __attribute__((ext_vector_type(2)));

constexpr int D = 640, H = 32, SK = 4096, DH = 192, F = 1536, HD = H * DH;
constexpr int NSPLIT = 8, KEYS = SK / NSPLIT;

__device__ __forceinline__ float bf2f(u16 b) { return __builtin_bit_cast(float, (unsigned)b << 16); }
__device__ __forceinline__ float rbf(float f) {
  return (float)(__bf16)f;
}
__device__ __forceinline__ u16 f2bf(float f) { return __builtin_bit_cast(u16, (__bf16)f); }
__device__ __forceinline__ float dot2(unsigned a, unsigned b, float c) {
  return __builtin_amdgcn_fdot2_f32_bf16(__builtin_bit_cast(bf16x2, a), __builtin_bit_cast(bf16x2, b), c, false);
}
__device__ __forceinline__ float wsum(float v) {
  for (int o = 32; o > 0; o >>= 1) v += __shfl_xor(v, o);
  return v;
}
__device__ __forceinline__ float wmax(float v) {
  for (int o = 32; o > 0; o >>= 1) v = fmaxf(v, __shfl_xor(v, o));
  return v;
}

__device__ __forceinline__ void layernorm(const u16* x, float* xn_lds, int lane) {
  float v[10]; float s = 0.f;
  for (int i = 0; i < 10; ++i) { v[i] = bf2f(x[lane + 64 * i]); s += v[i]; }
  s = wsum(s);
  float mean = s * (1.0f / D);
  float q = 0.f;
  for (int i = 0; i < 10; ++i) { float d = v[i] - mean; q += d * d; }
  q = wsum(q);
  float var = q * (1.0f / D);
  float den = sqrtf(var + 1e-5f);
  for (int i = 0; i < 10; ++i) xn_lds[lane + 64 * i] = (v[i] - mean) / den;
}

__global__ void __launch_bounds__(256) k1(const u16* x, const u16* wq, const u16* wg, const u16* wu, float* qout, float* hout) {
  __shared__ float xn[D];
  __shared__ float red[2][4][64];
  int tid = threadIdx.x, lane = tid & 63, w = tid >> 6;
  if (w == 0) layernorm(x, xn, lane);
  __syncthreads();
  int b = blockIdx.x;
  if (b < 96) {
    int c = b * 64 + lane; float a = 0.f;
    for (int k = w; k < D; k += 4) a += xn[k] * bf2f(wq[(size_t)k * HD + c]);
    red[0][w][lane] = a;
    __syncthreads();
    if (w == 0) { float r = red[0][0][lane] + red[0][1][lane] + red[0][2][lane] + red[0][3][lane];
      qout[c] = r; }
  } else {
    int c = (b - 96) * 64 + lane; float a = 0.f, u = 0.f;
    for (int k = w; k < D; k += 4) { a += xn[k] * bf2f(wg[(size_t)k * F + c]); u += xn[k] * bf2f(wu[(size_t)k * F + c]); }
    red[0][w][lane] = a; red[1][w][lane] = u;
    __syncthreads();
    if (w == 0) { float g = red[0][0][lane] + red[0][1][lane] + red[0][2][lane] + red[0][3][lane];
      float uu = red[1][0][lane] + red[1][1][lane] + red[1][2][lane] + red[1][3][lane];
      hout[c] = uu / (1.0f + __expf(-g));
    }
  }
}

__global__ void __launch_bounds__(512) k2(const u16* K, const u16* V, const float* q, float* po, float* pml) {
  __shared__ float sc[KEYS];
  __shared__ float red[8][DH];
  __shared__ float wm[8], wl[8];
  int tid = threadIdx.x, lane = tid & 63, w = tid >> 6;
  int h = blockIdx.x / NSPLIT, s = blockIdx.x % NSPLIT;
  int r = lane >> 3, c = lane & 7;
  float qf[24];
  for (int u = 0; u < 3; ++u) for (int e = 0; e < 8; ++e) qf[u * 8 + e] = q[h * DH + (c + 8 * u) * 8 + e];
  const u16* kb = K + ((size_t)h * SK + s * KEYS + w * 64) * DH;
  const u16* vb = V + ((size_t)h * SK + s * KEYS + w * 64) * DH;
  const float scale = 0.07216878364870322f;
  float m = -INFINITY;
  float sv[8];
#pragma unroll
  for (int t = 0; t < 8; ++t) {
    float a = 0.f;
#pragma unroll
    for (int u = 0; u < 3; ++u) {
      v4u kk = *(const v4u*)(kb + (size_t)(8 * t + r) * DH + (c + 8 * u) * 8);
#pragma unroll
      for (int e = 0; e < 4; ++e) {
        a += qf[u * 8 + 2 * e] * __builtin_bit_cast(float, kk[e] << 16);
        a += qf[u * 8 + 2 * e + 1] * __builtin_bit_cast(float, kk[e] & 0xffff0000u);
      }
    }
    a += __shfl_xor(a, 1); a += __shfl_xor(a, 2); a += __shfl_xor(a, 4);
    a = a * scale;
    sv[t] = a; m = fmaxf(m, a);
  }
  m = wmax(m);
  float l = 0.f;
  float acc[24];
  for (int i = 0; i < 24; ++i) acc[i] = 0.f;
#pragma unroll
  for (int t = 0; t < 8; ++t) {
    float p = __expf(sv[t] - m);
    l += p;
#pragma unroll
    for (int u = 0; u < 3; ++u) {
      v4u vv = *(const v4u*)(vb + (size_t)(8 * t + r) * DH + (c + 8 * u) * 8);
#pragma unroll
      for (int e = 0; e < 4; ++e) {
        acc[u * 8 + 2 * e] += p * __builtin_bit_cast(float, vv[e] << 16);
        acc[u * 8 + 2 * e + 1] += p * __builtin_bit_cast(float, vv[e] & 0xffff0000u);
      }
    }
  }
  l = wsum(l) * 0.125f;
  for (int i = 0; i < 24; ++i) { float a = acc[i]; a += __shfl_xor(a, 8); a += __shfl_xor(a, 16); a += __shfl_xor(a, 32); acc[i] = a; }
  if (r == 0) for (int u = 0; u < 3; ++u) for (int e = 0; e < 8; ++e) red[w][(c + 8 * u) * 8 + e] = acc[u * 8 + e];
  if (lane == 0) { wm[w] = m; wl[w] = l; }
  __syncthreads();
  if (tid < DH) {
    float M = -INFINITY;
    for (int i = 0; i < 8; ++i) M = fmaxf(M, wm[i]);
    float L = 0.f, o = 0.f;
    for (int i = 0; i < 8; ++i) { float f = __expf(wm[i] - M); L += f * wl[i]; o += f * red[i][tid]; }
    po[(size_t)blockIdx.x * DH + tid] = o;
    if (tid == 0) { pml[blockIdx.x * 2] = M; pml[blockIdx.x * 2 + 1] = L; }
  }
}

__global__ void __launch_bounds__(640) k3(const float* po, const float* pml, const float* hv, const u16* wo, const u16* wd, float* y) {
  __shared__ float o[DH];
  int j = blockIdx.x, tid = threadIdx.x;
  if (tid < DH) {
    if (j < H) {
      float M = -INFINITY;
      for (int i = 0; i < NSPLIT; ++i) M = fmaxf(M, pml[(j * NSPLIT + i) * 2]);
      float L = 0.f, a = 0.f;
      for (int i = 0; i < NSPLIT; ++i) { float f = __expf(pml[(j * NSPLIT + i) * 2] - M); L += f * pml[(j * NSPLIT + i) * 2 + 1]; a += f * po[(size_t)(j * NSPLIT + i) * DH + tid]; }
      a = a / L;
      o[tid] = a;
    } else o[tid] = hv[(j - H) * DH + tid];
  }
  __syncthreads();
  const u16* wb = j < H ? wo + (size_t)j * DH * D : wd + (size_t)(j - H) * DH * D;
  float a = 0.f;
  for (int k = 0; k < DH; ++k) a += o[k] * bf2f(wb[(size_t)k * D + tid]);
  y[j * D + tid] = a;
}

__global__ void __launch_bounds__(640) k4(const float* y, const u16* x, u16* out) {
  int tid = threadIdx.x;
  float a = 0.f, b = 0.f;
  for (int j = 0; j < H; ++j) a += y[j * D + tid];
  for (int j = H; j < H + 8; ++j) b += y[j * D + tid];
  float r = bf2f(x[tid]) + a + b;
  out[tid] = f2bf(r);
}

extern "C" void kernel_launch(void* const* d_in, const int* in_sizes, int n_in, void* d_out, int out_size, void* d_ws, size_t ws_size, hipStream_t stream) {
  const u16* x = (const u16*)d_in[0];
  const u16* K = (const u16*)d_in[1];
  const u16* V = (const u16*)d_in[2];
  const u16* wq = (const u16*)d_in[3];
  const u16* wo = (const u16*)d_in[4];
  const u16* wg = (const u16*)d_in[6];
  const u16* wu = (const u16*)d_in[7];
  const u16* wd = (const u16*)d_in[8];
  float* ws = (float*)d_ws;
  float* q = ws; float* hv = ws + 8192; float* po = ws + 16384; float* pml = ws + 16384 + 256 * 192; float* y = ws + 131072;
  k1<<<120, 256, 0, stream>>>(x, wq, wg, wu, q, hv);
  k2<<<256, 512, 0, stream>>>(K, V, q, po, pml);
  k3<<<40, 640, 0, stream>>>(po, pml, hv, wo, wd, y);
  k4<<<1, 640, 0, stream>>>(y, x, (u16*)d_out);
}
```

```cpp
#include <hip/hip_runtime.h>
#include <stdint.h>

namespace {

typedef unsigned short u16;
typedef unsigned long long u64;
typedef unsigned v4u __attribute__((ext_vector_type(4)));
typedef unsigned v2u __attribute__((ext_vector_type(2)));
typedef float v4f __attribute__((ext_vector_type(4)));
typedef __bf16 bf16x8 __attribute__((ext_vector_type(8)));
typedef __bf16 bf16x2 __attribute__((ext_vector_type(2)));
#define GAS __attribute__((address_space(1)))
#define LAS __attribute__((address_space(3)))
typedef GAS u64 gu64;
typedef GAS unsigned gu32;
#define RLX_AGENT __ATOMIC_RELAXED, __HIP_MEMORY_SCOPE_AGENT

constexpr int D = 640, H = 32, SK = 4096, DH = 192, F = 1536, HD = H * DH;
constexpr int NWG = 256, NTHR = 512;

#define LOOKAHEAD 14
#define KV_AUX 2
#define W_AUX 0
#define ROT 1

constexpr int WS_EPOCH = 0;
constexpr int WS_G0 = 4096;
constexpr int WS_G1 = WS_G0 + NWG * 1024;
constexpr int WS_G2 = WS_G1 + NWG * 1536;
constexpr int WS_STAMP = WS_G2 + NWG * 640;
constexpr unsigned TAG8 = 0xA5u;
constexpr unsigned SPIN_CAP = 1u << 15;

constexpr int L_XX = 0;
constexpr int L_RQ = L_XX + 2560;
constexpr int L_RG = L_RQ + 12288;
constexpr int L_QB = L_RG + 3072;
constexpr int L_ML = L_QB + 384;
constexpr int L_RO = L_ML + 64;
constexpr int L_RY = L_RO + 12288;
constexpr int L_END = L_RY + 5120;
constexpr int LDS_BYTES = 84 * 1024;
static_assert(L_END <= LDS_BYTES, "lds");

enum { WQ = 0, KK = 1, GU = 2, VV = 3, WO = 4, SM = 5 };
struct Item { int kind, idx; };
constexpr int NS = 30 + 24 + 3 + 24 + 1 + 8;
constexpr Item item(int n) {
  const int seg[][3] = {{WQ, 0, 6}, {KK, 0, 4}, {WQ, 6, 6}, {KK, 4, 3}, {WQ, 12, 6}, {KK, 7, 3}, {WQ, 18, 6}, {KK, 10, 3},
                        {WQ, 24, 6}, {GU, 0, 3}, {KK, 13, 11}, {VV, 0, 24}, {SM, 0, 1}, {WO, 6, 2}, {WO, 0, 6}};
  for (int i = 0; i < 15; ++i) {
    if (n < seg[i][2]) return Item{seg[i][0], seg[i][1] + n};
    n -= seg[i][2];
  }
  return Item{-1, -1};
}
constexpr int pos_of(int kind, int idx) {
  for (int n = 0; n < NS; ++n) if (item(n).kind == kind && item(n).idx == idx) return n;
  return -1;
}
constexpr int imin(int a, int b) { return a < b ? a : b; }
constexpr int imax(int a, int b) { return a > b ? a : b; }

__device__ __forceinline__ float dot2(unsigned a, unsigned b, float c) {
  return __builtin_amdgcn_fdot2_f32_bf16(__builtin_bit_cast(bf16x2, a), __builtin_bit_cast(bf16x2, b), c, false);
}
__device__ __forceinline__ v4f mfma(v4u a, v4u b, v4f c) {
  return __builtin_amdgcn_mfma_f32_16x16x32_bf16(__builtin_bit_cast(bf16x8, a), __builtin_bit_cast(bf16x8, b), c, 0, 0, 0);
}
__device__ __forceinline__ unsigned pk2(float lo, float hi) {
  bf16x2 p = {(__bf16)lo, (__bf16)hi};
  return __builtin_bit_cast(unsigned, p);
}
__device__ __forceinline__ float rbf(float f) { return (float)(__bf16)f; }
__device__ __forceinline__ float f_lo(unsigned u) { return __builtin_bit_cast(float, u << 16); }
__device__ __forceinline__ float f_hi(unsigned u) { return __builtin_bit_cast(float, u & 0xffff0000u); }
template <int CTRL> __device__ __forceinline__ float dpp(float x) {
  return __builtin_bit_cast(float, __builtin_amdgcn_mov_dpp(__builtin_bit_cast(int, x), CTRL, 0xf, 0xf, true));
}
constexpr int XOR1 = 0xB1, XOR2 = 0x4E, HMIR = 0x141, RMIR = 0x140;
__device__ __forceinline__ float sum8(float v) { v += dpp<XOR1>(v); v += dpp<XOR2>(v); v += dpp<HMIR>(v); return v; }
__device__ __forceinline__ float max8(float v) { v = fmaxf(v, dpp<XOR1>(v)); v = fmaxf(v, dpp<XOR2>(v)); v = fmaxf(v, dpp<HMIR>(v)); return v; }
__device__ __forceinline__ float x16_sum(float x) {
  auto s = __builtin_amdgcn_permlane16_swap(__float_as_uint(x), __float_as_uint(x), false, false);
  return __uint_as_float(s[0]) + __uint_as_float(s[1]);
}
__device__ __forceinline__ float x32_sum(float x) {
  auto s = __builtin_amdgcn_permlane32_swap(__float_as_uint(x), __float_as_uint(x), false, false);
  return __uint_as_float(s[0]) + __uint_as_float(s[1]);
}
__device__ __forceinline__ float x16_max(float x) {
  auto s = __builtin_amdgcn_permlane16_swap(__float_as_uint(x), __float_as_uint(x), false, false);
  return fmaxf(__uint_as_float(s[0]), __uint_as_float(s[1]));
}
__device__ __forceinline__ float x32_max(float x) {
  auto s = __builtin_amdgcn_permlane32_swap(__float_as_uint(x), __float_as_uint(x), false, false);
  return fmaxf(__uint_as_float(s[0]), __uint_as_float(s[1]));
}
__device__ __forceinline__ float wave_sum(float v) { v = sum8(v); v += dpp<RMIR>(v); return x32_sum(x16_sum(v)); }
__device__ __forceinline__ float grp_sum(float v) { v += dpp<RMIR>(v); return x32_sum(x16_sum(v)); }
__device__ __forceinline__ float grp_max(float v) { v = fmaxf(v, dpp<RMIR>(v)); return x32_max(x16_max(v)); }

__device__ __forceinline__ u64 granule(float v, unsigned tag) {
  return ((u64)tag << 32) | (u64)((__float_as_uint(v) & 0xffffff00u) | TAG8);
}
__device__ __forceinline__ unsigned gbad(u64 g, unsigned tag) { return ((unsigned)(g >> 32) ^ tag) | (((unsigned)g ^ TAG8) & 0xffu); }
__device__ __forceinline__ float gval(u64 g) { return __uint_as_float((unsigned)g & 0xffffff00u); }
__device__ __forceinline__ void wg_barrier() {
  asm volatile("s_waitcnt lgkmcnt(0)" ::: "memory");
  __builtin_amdgcn_s_barrier();
  asm volatile("" ::: "memory");
}
#define SB() __builtin_amdgcn_sched_barrier(0)
#define PIN1(x) asm volatile("" : "+v"(x))
#define PIN3(d) asm volatile("" : "+v"(d[0]), "+v"(d[1]), "+v"(d[2]))

#define STAMP(i) do { } while (0)

struct Args {
  const u16 *x, *k, *v, *wq, *wo, *wg, *wu, *wd;
  u16* out;
  unsigned char* ws;
};

struct Ctx {
  __amdgpu_buffer_rsrc_t rk, rv, rq, ro, rd, rx;
  const GAS unsigned char* gup;
  gu64* ge;
  int vq0, vq1, vk, vv, vo, vd, vx;
  int sq, skv, so, sd, rot, fidx, g1;
  v4u wq[30], k[24], v[24], gu[3], wo[8];
  u64 eg, eu, em, el;
  unsigned xf;
};

__device__ __forceinline__ int rot10(int t, int rot) { int a = t + rot; return a >= 10 ? a - 10 : a; }

template <int N> __device__ __forceinline__ void issue1(Ctx& c) {
  constexpr Item it = item(N);
  if constexpr (it.kind == WQ) {
    constexpr int t = it.idx / 3, u = it.idx % 3;
    const int so = c.sq + rot10(t, c.rot) * (HD * 2) + (u == 2 ? 10 * HD * 2 + 128 : 0);
    c.wq[it.idx] = __builtin_amdgcn_raw_buffer_load_b128(c.rq, u == 1 ? c.vq1 : c.vq0, so, W_AUX);
  } else if constexpr (it.kind == KK) {
    constexpr int t = it.idx / 3, u = it.idx % 3;
    c.k[it.idx] = __builtin_amdgcn_raw_buffer_load_b128(c.rk, c.vk + 128 * u, c.skv + 3072 * t, KV_AUX);
  } else if constexpr (it.kind == VV) {
    constexpr int t = it.idx / 3, u = it.idx % 3;
    c.v[it.idx] = __builtin_amdgcn_raw_buffer_load_b128(c.rv, c.vv + 256 * u, c.skv + 3072 * t, KV_AUX);
  } else if constexpr (it.kind == GU) {
    const GAS unsigned char* p = c.gup + (it.idx == 2 ? c.g1 : it.idx * 4 * F * 2);
    c.gu[it.idx] = *(const GAS v4u*)p;
  } else if constexpr (it.kind == WO) {
    if constexpr (it.idx < 6) c.wo[it.idx] = __builtin_amdgcn_raw_buffer_load_b128(c.ro, c.vo, c.so + it.idx * (D * 2), W_AUX);
    else c.wo[it.idx] = __builtin_amdgcn_raw_buffer_load_b128(c.rd, c.vd, c.sd + (it.idx - 6) * (D * 2), W_AUX);
  } else if constexpr (it.kind == SM) {
    c.eg = __hip_atomic_load(c.ge + c.fidx, RLX_AGENT);
    c.eu = __hip_atomic_load(c.ge + 48 + c.fidx, RLX_AGENT);
    c.em = __hip_atomic_load(c.ge + 96, RLX_AGENT);
    c.el = __hip_atomic_load(c.ge + 97, RLX_AGENT);
    c.xf = __builtin_amdgcn_raw_buffer_load_b16(c.rx, c.vx, 0, 0);
  }
}
template <int LO, int HI> __device__ __forceinline__ void issue(Ctx& c) {
  if constexpr (LO < HI && LO < NS) {
    issue1<LO>(c);
    SB();
    issue<LO + 1, HI>(c);
  }
}
constexpr int issued_after(int p) { return imin(NS, p + 1 + LOOKAHEAD); }

__device__ __forceinline__ v4u diag(unsigned xx, const unsigned (&mk)[4]) { return v4u{xx & mk[0], xx & mk[1], xx & mk[2], xx & mk[3]}; }

template <int T> __device__ __forceinline__ void q_step(Ctx& c, const unsigned (&xa)[10], const unsigned (&mk)[4], v4f (&dq)[3]) {
  if constexpr (T < 10) {
    const v4u a = diag(xa[T], mk);
#pragma unroll
    for (int u = 0; u < 3; ++u) dq[u] = mfma(a, c.wq[3 * T + u], dq[u]);
    PIN3(dq);
    SB();
    constexpr int prev = T == 0 ? LOOKAHEAD : issued_after(pos_of(WQ, 3 * T - 1));
    issue<prev, issued_after(pos_of(WQ, 3 * T + 2))>(c);
    q_step<T + 1>(c, xa, mk, dq);
  }
}
template <int T> __device__ __forceinline__ void k_step(Ctx& c, const v4u (&q)[3], float (&sc)[8]) {
  if constexpr (T < 8) {
    float a = 0.f;
#pragma unroll
    for (int u = 0; u < 3; ++u)
#pragma unroll
      for (int e = 0; e < 4; ++e) a = dot2(c.k[3 * T + u][e], q[u][e], a);
    PIN1(a);
    sc[T] = a;
    constexpr int base = issued_after(pos_of(GU, 2));
    constexpr int prev = T == 0 ? base : imax(base, issued_after(pos_of(KK, 3 * T - 1)));
    constexpr int next = imax(base, issued_after(pos_of(KK, 3 * T + 2)));
    if constexpr (next > prev) { SB(); issue<prev, next>(c); }
    k_step<T + 1>(c, q, sc);
  }
}
template <int T> __device__ __forceinline__ void v_step(Ctx& c, const unsigned (&pp)[8], const unsigned (&mk)[4], v4f (&dv)[3]) {
  if constexpr (T < 8) {
    const v4u a = diag(pp[T], mk);
#pragma unroll
    for (int u = 0; u < 3; ++u) dv[u] = mfma(a, c.v[3 * T + u], dv[u]);
    PIN3(dv);
    SB();
    constexpr int prev = T == 0 ? issued_after(pos_of(KK, 23)) : issued_after(pos_of(VV, 3 * T - 1));
    issue<prev, issued_after(pos_of(VV, 3 * T + 2))>(c);
    v_step<T + 1>(c, pp, mk, dv);
  }
}

__device__ __forceinline__ void put192(LAS unsigned char* L, int off, const v4f (&d)[3], int w, int lane) {
  const int j = lane & 15, q = lane >> 4, hi = lane >> 5;
  const v4f a = hi ? d[2] : d[0];
  LAS float* base = (LAS float*)(L + off) + (2 * w + hi) * 192 + 8 * j + 4 * (q & 1);
  *(LAS v4f*)(base + 64 * hi) = a;
  if ((j >> 3) == hi) *(LAS v4f*)(base + (hi ? -64 : 128)) = d[1];
}

__global__ void __launch_bounds__(NTHR, 2) fused_kernel(Args a) {
  __shared__ __attribute__((aligned(16))) unsigned char smem[LDS_BYTES];
  LAS unsigned char* L = (LAS unsigned char*)smem;
  STAMP(0);
  const int tid = threadIdx.x, lane = tid & 63, w = __builtin_amdgcn_readfirstlane(tid >> 6);
  const int b = blockIdx.x, h = (b & 7) * 4 + (b >> 6), s = (b >> 3) & 7, wg = h * 8 + s;
  const int r = lane >> 3, cc = lane & 7, g = lane >> 4, j = lane & 15;

  Ctx c;
  c.rx = __builtin_amdgcn_make_buffer_rsrc((void*)a.x, 0, D * 2, 0x00020000);
  c.rq = __builtin_amdgcn_make_buffer_rsrc((void*)a.wq, 0, D * HD * 2, 0x00020000);
  c.rk = __builtin_amdgcn_make_buffer_rsrc((void*)a.k, 0, H * SK * DH * 2, 0x00020000);
  c.rv = __builtin_amdgcn_make_buffer_rsrc((void*)a.v, 0, H * SK * DH * 2, 0x00020000);
  c.ro = __builtin_amdgcn_make_buffer_rsrc((void*)a.wo, 0, HD * D * 2, 0x00020000);
  c.rd = __builtin_amdgcn_make_buffer_rsrc((void*)a.wd, 0, F * D * 2, 0x00020000);
  const v4u x4 = __builtin_amdgcn_raw_buffer_load_b128(c.rx, lane * 20, 0, 0);
  const unsigned x1 = __builtin_amdgcn_raw_buffer_load_b32(c.rx, lane * 20 + 16, 0, 0);
  const unsigned epoch = __hip_atomic_load((gu32*)(a.ws + WS_EPOCH), RLX_AGENT);
  c.vq0 = 20 * g * (HD * 2) + 16 * j;
  c.vq1 = c.vq0 + (j < 8 ? 256 : 10 * HD * 2 - 128);
  c.sq = 80 * w * (HD * 2) + h * (DH * 2);
  c.rot = ROT ? (s * 5) >> 2 : 0;
  c.vk = r * (DH * 2) + cc * 16;
  c.vv = g * (DH * 4) + j * 16;
  c.skv = (wg * 512 + w * 64) * (DH * 2);
  SB();
  issue<0, LOOKAHEAD>(c);
  STAMP(1);

  const int j9 = j < 9 ? j : 9;
  c.vo = 6 * g * (D * 2) + j9 * 16;
  c.so = (DH * h + 24 * w) * (D * 2) + 160 * s;
  c.vd = 2 * (g < 2 ? g : 2) * (D * 2) + j9 * 16;
  c.sd = (48 * h + 6 * w) * (D * 2) + 160 * s;
  c.gup = (const GAS unsigned char*)(j < 8 ? a.wg : a.wu) + (unsigned)((80 * s + 10 * w + g) * (F * 2) + h * 96 + (cc < 5 ? cc : 5) * 16);
  c.g1 = (g < 2 ? 8 : 6) * (F * 2);
  c.ge = (gu64*)(a.ws + WS_G0) + (h * 8 + cc) * 128;
  c.fidx = 6 * w + (r < 5 ? r : 5);
  const int cp = 5 * (h >> 2) + (w - 3);
  const int fcol = 80 * s + 2 * cp + (lane >> 5);
  c.vx = 2 * fcol;
  unsigned mk[4];
#pragma unroll
  for (int v = 0; v < 4; ++v) mk[v] = (cc >> 1) == v ? ((cc & 1) ? 0xffff0000u : 0x0000ffffu) : 0u;

  unsigned xa[10], xg[3];
  {
    float xv[10] = {f_lo(x4[0]), f_hi(x4[0]), f_lo(x4[1]), f_hi(x4[1]), f_lo(x4[2]), f_hi(x4[2]), f_lo(x4[3]), f_hi(x4[3]), f_lo(x1), f_hi(x1)};
    float sm = 0.f;
#pragma unroll
    for (int i = 0; i < 10; ++i) sm += xv[i];
    const float mean = wave_sum(sm) * (1.0f / D);
    float sq = 0.f;
#pragma unroll
    for (int i = 0; i < 10; ++i) { const float d = xv[i] - mean; sq = fmaf(d, d, sq); }
    const float var = wave_sum(sq) * (1.0f / D);
    const float m = rbf(mean);
    const float den = rbf(__builtin_sqrtf(rbf(rbf(var) + 1e-5f)));
    const float inv = 1.0f / den;
    LAS v2u* xw = (LAS v2u*)(L + L_XX + lane * 40);
#pragma unroll
    for (int i = 0; i < 5; ++i) {
      const float n0 = rbf(xv[2 * i] - m) * inv, n1 = rbf(xv[2 * i + 1] - m) * inv;
      xw[i] = v2u{pk2(n0, n0), pk2(n1, n1)};
    }
    asm volatile("s_waitcnt lgkmcnt(0)" ::: "memory");
    const LAS unsigned* xr = (const LAS unsigned*)(L + L_XX);
#pragma unroll
    for (int t = 0; t < 10; ++t) xa[t] = xr[80 * w + 10 * r + rot10(t, c.rot)];
    xg[0] = xr[80 * s + 10 * w + g];
    xg[1] = xr[80 * s + 10 * w + 4 + g];
    xg[2] = g < 2 ? xr[80 * s + 10 * w + 8 + (g & 1)] : 0u;
  }
  const unsigned tag = __builtin_amdgcn_readfirstlane(epoch) + 1u;
  STAMP(2);

  v4f dq[3] = {v4f{0, 0, 0, 0}, v4f{0, 0, 0, 0}, v4f{0, 0, 0, 0}};
  q_step<0>(c, xa, mk, dq);
  v4f dg = {0, 0, 0, 0};
#pragma unroll
  for (int n = 0; n < 3; ++n) dg = mfma(diag(xg[n], mk), c.gu[n], dg);
  PIN1(dg);
  SB();
  issue<issued_after(pos_of(WQ, 29)), issued_after(pos_of(GU, 2))>(c);
  put192(L, L_RQ, dq, w, lane);
  if (lane < 32 && cc < 6) *(LAS v4f*)((LAS float*)(L + L_RG) + w * 96 + (j >> 3) * 48 + 8 * cc + 4 * g) = dg;
  STAMP(3);
  wg_barrier();
  if (tid < 192) {
    const LAS float* p = (const LAS float*)(L + L_RQ) + tid;
    float acc = 0.f;
#pragma unroll
    for (int i = 0; i < 16; ++i) acc += p[i * 192];
    ((LAS __bf16*)(L + L_QB))[tid] = (__bf16)acc;
  } else if (tid < 288) {
    const LAS float* p = (const LAS float*)(L + L_RG) + (tid - 192);
    float acc = 0.f;
#pragma unroll
    for (int i = 0; i < 8; ++i) acc += p[i * 96];
    __hip_atomic_store((gu64*)(a.ws + WS_G0) + wg * 128 + (tid - 192), granule(acc, tag), RLX_AGENT);
  }
  wg_barrier();
  STAMP(4);

  v4u q[3];
#pragma unroll
  for (int u = 0; u < 3; ++u) q[u] = *(const LAS v4u*)(L + L_QB + 16 * (cc + 8 * u));
  float sc[8];
  k_step<0>(c, q, sc);
  constexpr float SCALE = 0.07216878364870322f, LOG2E = 1.4426950408889634f;
  float mw = -INFINITY;
#pragma unroll
  for (int t = 0; t < 8; ++t) { sc[t] = rbf(rbf(sum8(sc[t])) * SCALE); mw = fmaxf(mw, sc[t]); }
  mw = grp_max(mw);
  {
    float lw = 0.f;
#pragma unroll
    for (int t = 0; t < 8; ++t) lw += __builtin_amdgcn_exp2f((sc[t] - mw) * LOG2E);
    lw = grp_sum(lw);
    if (lane == 0) *(LAS v2u*)(L + L_ML + 8 * w) = v2u{__float_as_uint(mw), __float_as_uint(lw)};
  }
  STAMP(5);
  wg_barrier();
  unsigned pp[8];
  {
    const v2u ml = *(const LAS v2u*)(L + L_ML + 8 * cc);
    const float mc = __uint_as_float(ml[0]);
    const float M = max8(mc);
    const float Ls = sum8(__uint_as_float(ml[1]) * __builtin_amdgcn_exp2f((mc - M) * LOG2E));
    if (tid < 2) __hip_atomic_store((gu64*)(a.ws + WS_G0) + wg * 128 + 96 + tid, granule(tid ? Ls : M, tag), RLX_AGENT);
#pragma unroll
    for (int t = 0; t < 8; ++t) { const float p = __builtin_amdgcn_exp2f((sc[t] - M) * LOG2E); pp[t] = pk2(p, p); }
  }

  v4f dv[3] = {v4f{0, 0, 0, 0}, v4f{0, 0, 0, 0}, v4f{0, 0, 0, 0}};
  v_step<0>(c, pp, mk, dv);
  put192(L, L_RO, dv, w, lane);
  STAMP(6);
  wg_barrier();
  if (tid < 192) {
    const LAS float* p = (const LAS float*)(L + L_RO) + tid;
    float acc = 0.f;
#pragma unroll
    for (int i = 0; i < 16; ++i) acc += p[i * 192];
    __hip_atomic_store((gu64*)(a.ws + WS_G1) + wg * 192 + tid, granule(acc, tag), RLX_AGENT);
  }
  STAMP(7);

  for (unsigned spin = 0; spin < SPIN_CAP; ++spin) {
    const unsigned bad = gbad(c.eg, tag) | gbad(c.eu, tag) | gbad(c.em, tag) | gbad(c.el, tag);
    if (__all(bad == 0u)) break;
    c.eg = __hip_atomic_load(c.ge + c.fidx, RLX_AGENT);
    c.eu = __hip_atomic_load(c.ge + 48 + c.fidx, RLX_AGENT);
    c.em = __hip_atomic_load(c.ge + 96, RLX_AGENT);
    c.el = __hip_atomic_load(c.ge + 97, RLX_AGENT);
  }
  float fac;
  v4f ylo = {0, 0, 0, 0}, yhi = {0, 0, 0, 0};
  {
    const float ms = gval(c.em), M = max8(ms);
    const float e = __builtin_amdgcn_exp2f((ms - M) * LOG2E);
    fac = e / sum8(e * gval(c.el));
    const float gg = rbf(sum8(gval(c.eg))), uu = rbf(sum8(gval(c.eu)));
    const float sg = rbf(1.0f / (1.0f + __builtin_amdgcn_exp2f(-gg * LOG2E)));
    const float hh = r < 6 ? sg * uu : 0.f;
    const v4u ah = diag(pk2(hh, hh), mk);
    ylo = mfma(ah, c.wo[6], ylo);
    yhi = mfma(ah, c.wo[7], yhi);
  }
  {
    gu64* go = (gu64*)(a.ws + WS_G1) + (h * 8 + cc) * 192 + 24 * w + 3 * r;
    u64 o0, o1, o2;
    for (unsigned spin = 0;; ++spin) {
      o0 = __hip_atomic_load(go, RLX_AGENT);
      o1 = __hip_atomic_load(go + 1, RLX_AGENT);
      o2 = __hip_atomic_load(go + 2, RLX_AGENT);
      const unsigned bad = gbad(o0, tag) | gbad(o1, tag) | gbad(o2, tag);
      if (__all(bad == 0u) || spin >= SPIN_CAP) break;
    }
    STAMP(8);
    const float f0 = sum8(gval(o0) * fac), f1 = sum8(gval(o1) * fac), f2 = sum8(gval(o2) * fac);
    const v4u a0 = diag(pk2(f0, f0), mk), a1 = diag(pk2(f1, f1), mk), a2 = diag(pk2(f2, f2), mk);
    ylo = mfma(a0, c.wo[0], ylo); yhi = mfma(a0, c.wo[3], yhi);
    ylo = mfma(a1, c.wo[1], ylo); yhi = mfma(a1, c.wo[4], yhi);
    ylo = mfma(a2, c.wo[2], ylo); yhi = mfma(a2, c.wo[5], yhi);
    const v4f y = (lane >> 5) ? yhi : ylo;
    if (j < 10) *(LAS v4f*)((LAS float*)(L + L_RY) + (2 * w + (lane >> 5)) * 80 + 8 * j + 4 * (g & 1)) = y;
  }
  wg_barrier();
  STAMP(9);
  if (tid < 80) {
    const LAS float* p = (const LAS float*)(L + L_RY) + tid;
    float acc = 0.f;
#pragma unroll
    for (int i = 0; i < 16; ++i) acc += p[i * 80];
    __hip_atomic_store((gu64*)(a.ws + WS_G2) + wg * 80 + tid, granule(acc, tag), RLX_AGENT);
  }
  if ((h & 3) == 0 && w >= 3) {
    gu64* gy = (gu64*)(a.ws + WS_G2) + ((lane & 31) * 8 + s) * 80 + 2 * cp + (lane >> 5);
    u64 y;
    for (unsigned spin = 0;; ++spin) {
      y = __hip_atomic_load(gy, RLX_AGENT);
      if (__all(gbad(y, tag) == 0u) || spin >= SPIN_CAP) break;
    }
    float t = sum8(gval(y));
    t += dpp<RMIR>(t);
    t = x16_sum(t);
    t += f_lo(c.xf);
    if ((lane & 31) == 0) ((GAS __bf16*)a.out)[fcol] = (__bf16)t;
    if (wg == 0 && tid == 192) __hip_atomic_store((gu32*)(a.ws + WS_EPOCH), tag, RLX_AGENT);
  }
  STAMP(10);
}

}

extern "C" void kernel_launch(void* const* d_in, const int* in_sizes, int n_in, void* d_out, int out_size, void* d_ws, size_t ws_size,
                              hipStream_t stream) {
  Args a;
  a.x = (const u16*)d_in[0];
  a.k = (const u16*)d_in[1];
  a.v = (const u16*)d_in[2];
  a.wq = (const u16*)d_in[3];
  a.wo = (const u16*)d_in[4];
  a.wg = (const u16*)d_in[6];
  a.wu = (const u16*)d_in[7];
  a.wd = (const u16*)d_in[8];
  a.out = (u16*)d_out;
  a.ws = (unsigned char*)d_ws;
  fused_kernel<<<NWG, NTHR, 0, stream>>>(a);
}
```

```cpp
#define PROBE -1
#include <hip/hip_runtime.h>
#include <stdint.h>

namespace {

typedef unsigned short u16;
typedef unsigned long long u64;
typedef unsigned v4u __attribute__((ext_vector_type(4)));
typedef unsigned v2u __attribute__((ext_vector_type(2)));
typedef float v4f __attribute__((ext_vector_type(4)));
typedef __bf16 bf16x8 __attribute__((ext_vector_type(8)));
typedef __bf16 bf16x2 __attribute__((ext_vector_type(2)));
#define GAS __attribute__((address_space(1)))
#define LAS __attribute__((address_space(3)))
typedef GAS u64 gu64;
typedef GAS unsigned gu32;
#define RLX_AGENT __ATOMIC_RELAXED, __HIP_MEMORY_SCOPE_AGENT

constexpr int D = 640, H = 32, SK = 4096, DH = 192, F = 1536, HD = H * DH;
constexpr int NWG = 256, NTHR = 512;

#define LOOKAHEAD 14
#define KV_AUX 2
#define W_AUX 0
#define ROT 1

constexpr int WS_EPOCH = 0;
constexpr int WS_G0 = 4096;
constexpr int WS_G1 = WS_G0 + NWG * 1024;
constexpr int WS_G2 = WS_G1 + NWG * 1536;
constexpr int WS_STAMP = WS_G2 + NWG * 640;
constexpr unsigned TAG8 = 0xA5u;
constexpr unsigned SPIN_CAP = 1u << 15;

constexpr int L_XX = 0;
constexpr int L_RQ = L_XX + 2560;
constexpr int L_RG = L_RQ + 12288;
constexpr int L_QB = L_RG + 3072;
constexpr int L_ML = L_QB + 384;
constexpr int L_RO = L_ML + 64;
constexpr int L_RY = L_RO + 12288;
constexpr int L_END = L_RY + 5120;
constexpr int LDS_BYTES = 84 * 1024;
static_assert(L_END <= LDS_BYTES, "lds");

enum { WQ = 0, KK = 1, GU = 2, VV = 3, WO = 4, SM = 5 };
struct Item { int kind, idx; };
constexpr int NS = 30 + 24 + 3 + 24 + 1 + 8;
constexpr Item item(int n) {
  const int seg[][3] = {{WQ, 0, 6}, {KK, 0, 4}, {WQ, 6, 6}, {KK, 4, 3}, {WQ, 12, 6}, {KK, 7, 3}, {WQ, 18, 6}, {KK, 10, 3},
                        {WQ, 24, 6}, {GU, 0, 3}, {KK, 13, 11}, {VV, 0, 24}, {SM, 0, 1}, {WO, 6, 2}, {WO, 0, 6}};
  for (int i = 0; i < 15; ++i) {
    if (n < seg[i][2]) return Item{seg[i][0], seg[i][1] + n};
    n -= seg[i][2];
  }
  return Item{-1, -1};
}
constexpr int pos_of(int kind, int idx) {
  for (int n = 0; n < NS; ++n) if (item(n).kind == kind && item(n).idx == idx) return n;
  return -1;
}
constexpr int imin(int a, int b) { return a < b ? a : b; }
constexpr int imax(int a, int b) { return a > b ? a : b; }

__device__ __forceinline__ float dot2(unsigned a, unsigned b, float c) {
  return __builtin_amdgcn_fdot2_f32_bf16(__builtin_bit_cast(bf16x2, a), __builtin_bit_cast(bf16x2, b), c, false);
}
__device__ __forceinline__ v4f mfma(v4u a, v4u b, v4f c) {
  return __builtin_amdgcn_mfma_f32_16x16x32_bf16(__builtin_bit_cast(bf16x8, a), __builtin_bit_cast(bf16x8, b), c, 0, 0, 0);
}
__device__ __forceinline__ unsigned pk2(float lo, float hi) {
  bf16x2 p = {(__bf16)lo, (__bf16)hi};
  return __builtin_bit_cast(unsigned, p);
}
__device__ __forceinline__ float rbf(float f) { return (float)(__bf16)f; }
__device__ __forceinline__ float f_lo(unsigned u) { return __builtin_bit_cast(float, u << 16); }
__device__ __forceinline__ float f_hi(unsigned u) { return __builtin_bit_cast(float, u & 0xffff0000u); }
template <int CTRL> __device__ __forceinline__ float dpp(float x) {
  return __builtin_bit_cast(float, __builtin_amdgcn_mov_dpp(__builtin_bit_cast(int, x), CTRL, 0xf, 0xf, true));
}
constexpr int XOR1 = 0xB1, XOR2 = 0x4E, HMIR = 0x141, RMIR = 0x140;
__device__ __forceinline__ float sum8(float v) { v += dpp<XOR1>(v); v += dpp<XOR2>(v); v += dpp<HMIR>(v); return v; }
__device__ __forceinline__ float max8(float v) { v = fmaxf(v, dpp<XOR1>(v)); v = fmaxf(v, dpp<XOR2>(v)); v = fmaxf(v, dpp<HMIR>(v)); return v; }
__device__ __forceinline__ float x16_sum(float x) {
  auto s = __builtin_amdgcn_permlane16_swap(__float_as_uint(x), __float_as_uint(x), false, false);
  return __uint_as_float(s[0]) + __uint_as_float(s[1]);
}
__device__ __forceinline__ float x32_sum(float x) {
  auto s = __builtin_amdgcn_permlane32_swap(__float_as_uint(x), __float_as_uint(x), false, false);
  return __uint_as_float(s[0]) + __uint_as_float(s[1]);
}
__device__ __forceinline__ float x16_max(float x) {
  auto s = __builtin_amdgcn_permlane16_swap(__float_as_uint(x), __float_as_uint(x), false, false);
  return fmaxf(__uint_as_float(s[0]), __uint_as_float(s[1]));
}
__device__ __forceinline__ float x32_max(float x) {
  auto s = __builtin_amdgcn_permlane32_swap(__float_as_uint(x), __float_as_uint(x), false, false);
  return fmaxf(__uint_as_float(s[0]), __uint_as_float(s[1]));
}
__device__ __forceinline__ float wave_sum(float v) { v = sum8(v); v += dpp<RMIR>(v); return x32_sum(x16_sum(v)); }
__device__ __forceinline__ float grp_sum(float v) { v += dpp<RMIR>(v); return x32_sum(x16_sum(v)); }
__device__ __forceinline__ float grp_max(float v) { v = fmaxf(v, dpp<RMIR>(v)); return x32_max(x16_max(v)); }

__device__ __forceinline__ u64 granule(float v, unsigned tag) {
  return ((u64)tag << 32) | (u64)((__float_as_uint(v) & 0xffffff00u) | TAG8);
}
__device__ __forceinline__ unsigned gbad(u64 g, unsigned tag) { return ((unsigned)(g >> 32) ^ tag) | (((unsigned)g ^ TAG8) & 0xffu); }
__device__ __forceinline__ float gval(u64 g) { return __uint_as_float((unsigned)g & 0xffffff00u); }
__device__ __forceinline__ void wg_barrier() {
  asm volatile("s_waitcnt lgkmcnt(0)" ::: "memory");
  __builtin_amdgcn_s_barrier();
  asm volatile("" ::: "memory");
}
#define SB() __builtin_amdgcn_sched_barrier(0)
#define PIN1(x) asm volatile("" : "+v"(x))
#define PIN3(d) asm volatile("" : "+v"(d[0]), "+v"(d[1]), "+v"(d[2]))

#define STAMPS
#define NSTAMP 11
#define STAMP(i) do { SB(); st[i] = __builtin_amdgcn_s_memrealtime(); SB(); } while (0)

struct Args {
  const u16 *x, *k, *v, *wq, *wo, *wg, *wu, *wd;
  u16* out;
  unsigned char* ws;
};

struct Ctx {
  __amdgpu_buffer_rsrc_t rk, rv, rq, ro, rd, rx;
  const GAS unsigned char* gup;
  gu64* ge;
  int vq0, vq1, vk, vv, vo, vd, vx;
  int sq, skv, so, sd, rot, fidx, g1;
  v4u wq[30], k[24], v[24], gu[3], wo[8];
  u64 eg, eu, em, el;
  unsigned xf;
};

__device__ __forceinline__ int rot10(int t, int rot) { int a = t + rot; return a >= 10 ? a - 10 : a; }

template <int N> __device__ __forceinline__ void issue1(Ctx& c) {
  constexpr Item it = item(N);
  if constexpr (it.kind == WQ) {
    constexpr int t = it.idx / 3, u = it.idx % 3;
    const int so = c.sq + rot10(t, c.rot) * (HD * 2) + (u == 2 ? 10 * HD * 2 + 128 : 0);
    c.wq[it.idx] = __builtin_amdgcn_raw_buffer_load_b128(c.rq, u == 1 ? c.vq1 : c.vq0, so, W_AUX);
  } else if constexpr (it.kind == KK) {
    constexpr int t = it.idx / 3, u = it.idx % 3;
    c.k[it.idx] = __builtin_amdgcn_raw_buffer_load_b128(c.rk, c.vk + 128 * u, c.skv + 3072 * t, KV_AUX);
  } else if constexpr (it.kind == VV) {
    constexpr int t = it.idx / 3, u = it.idx % 3;
    c.v[it.idx] = __builtin_amdgcn_raw_buffer_load_b128(c.rv, c.vv + 256 * u, c.skv + 3072 * t, KV_AUX);
  } else if constexpr (it.kind == GU) {
    const GAS unsigned char* p = c.gup + (it.idx == 2 ? c.g1 : it.idx * 4 * F * 2);
    c.gu[it.idx] = *(const GAS v4u*)p;
  } else if constexpr (it.kind == WO) {
    if constexpr (it.idx < 6) c.wo[it.idx] = __builtin_amdgcn_raw_buffer_load_b128(c.ro, c.vo, c.so + it.idx * (D * 2), W_AUX);
    else c.wo[it.idx] = __builtin_amdgcn_raw_buffer_load_b128(c.rd, c.vd, c.sd + (it.idx - 6) * (D * 2), W_AUX);
  } else if constexpr (it.kind == SM) {
    c.eg = __hip_atomic_load(c.ge + c.fidx, RLX_AGENT);
    c.eu = __hip_atomic_load(c.ge + 48 + c.fidx, RLX_AGENT);
    c.em = __hip_atomic_load(c.ge + 96, RLX_AGENT);
    c.el = __hip_atomic_load(c.ge + 97, RLX_AGENT);
    c.xf = __builtin_amdgcn_raw_buffer_load_b16(c.rx, c.vx, 0, 0);
  }
}
template <int LO, int HI> __device__ __forceinline__ void issue(Ctx& c) {
  if constexpr (LO < HI && LO < NS) {
    issue1<LO>(c);
    SB();
    issue<LO + 1, HI>(c);
  }
}
constexpr int issued_after(int p) { return imin(NS, p + 1 + LOOKAHEAD); }

__device__ __forceinline__ v4u diag(unsigned xx, const unsigned (&mk)[4]) { return v4u{xx & mk[0], xx & mk[1], xx & mk[2], xx & mk[3]}; }

template <int T> __device__ __forceinline__ void q_step(Ctx& c, const unsigned (&xa)[10], const unsigned (&mk)[4], v4f (&dq)[3]) {
  if constexpr (T < 10) {
    const v4u a = diag(xa[T], mk);
#pragma unroll
    for (int u = 0; u < 3; ++u) dq[u] = mfma(a, c.wq[3 * T + u], dq[u]);
    PIN3(dq);
    SB();
    constexpr int prev = T == 0 ? LOOKAHEAD : issued_after(pos_of(WQ, 3 * T - 1));
    issue<prev, issued_after(pos_of(WQ, 3 * T + 2))>(c);
    q_step<T + 1>(c, xa, mk, dq);
  }
}
template <int T> __device__ __forceinline__ void k_step(Ctx& c, const v4u (&q)[3], float (&sc)[8]) {
  if constexpr (T < 8) {
    float a = 0.f;
#pragma unroll
    for (int u = 0; u < 3; ++u)
#pragma unroll
      for (int e = 0; e < 4; ++e) a = dot2(c.k[3 * T + u][e], q[u][e], a);
    PIN1(a);
    sc[T] = a;
    constexpr int base = issued_after(pos_of(GU, 2));
    constexpr int prev = T == 0 ? base : imax(base, issued_after(pos_of(KK, 3 * T - 1)));
    constexpr int next = imax(base, issued_after(pos_of(KK, 3 * T + 2)));
    if constexpr (next > prev) { SB(); issue<prev, next>(c); }
    k_step<T + 1>(c, q, sc);
  }
}
template <int T> __device__ __forceinline__ void v_step(Ctx& c, const unsigned (&pp)[8], const unsigned (&mk)[4], v4f (&dv)[3]) {
  if constexpr (T < 8) {
    const v4u a = diag(pp[T], mk);
#pragma unroll
    for (int u = 0; u < 3; ++u) dv[u] = mfma(a, c.v[3 * T + u], dv[u]);
    PIN3(dv);
    SB();
    constexpr int prev = T == 0 ? issued_after(pos_of(KK, 23)) : issued_after(pos_of(VV, 3 * T - 1));
    issue<prev, issued_after(pos_of(VV, 3 * T + 2))>(c);
    v_step<T + 1>(c, pp, mk, dv);
  }
}

__device__ __forceinline__ void put192(LAS unsigned char* L, int off, const v4f (&d)[3], int w, int lane) {
  const int j = lane & 15, q = lane >> 4, hi = lane >> 5;
  const v4f a = hi ? d[2] : d[0];
  LAS float* base = (LAS float*)(L + off) + (2 * w + hi) * 192 + 8 * j + 4 * (q & 1);
  *(LAS v4f*)(base + 64 * hi) = a;
  if ((j >> 3) == hi) *(LAS v4f*)(base + (hi ? -64 : 128)) = d[1];
}

__global__ void __launch_bounds__(NTHR, 2) fused_kernel(Args a) {
  __shared__ __attribute__((aligned(16))) unsigned char smem[LDS_BYTES];
  LAS unsigned char* L = (LAS unsigned char*)smem;
  u64 st[12];
  STAMP(0);
  const int tid = threadIdx.x, lane = tid & 63, w = __builtin_amdgcn_readfirstlane(tid >> 6);
  const int b = blockIdx.x, h = (b & 7) * 4 + (b >> 6), s = (b >> 3) & 7, wg = h * 8 + s;
  const int r = lane >> 3, cc = lane & 7, g = lane >> 4, j = lane & 15;

  Ctx c;
  c.rx = __builtin_amdgcn_make_buffer_rsrc((void*)a.x, 0, D * 2, 0x00020000);
  c.rq = __builtin_amdgcn_make_buffer_rsrc((void*)a.wq, 0, D * HD * 2, 0x00020000);
  c.rk = __builtin_amdgcn_make_buffer_rsrc((void*)a.k, 0, H * SK * DH * 2, 0x00020000);
  c.rv = __builtin_amdgcn_make_buffer_rsrc((void*)a.v, 0, H * SK * DH * 2, 0x00020000);
  c.ro = __builtin_amdgcn_make_buffer_rsrc((void*)a.wo, 0, HD * D * 2, 0x00020000);
  c.rd = __builtin_amdgcn_make_buffer_rsrc((void*)a.wd, 0, F * D * 2, 0x00020000);
  const v4u x4 = __builtin_amdgcn_raw_buffer_load_b128(c.rx, lane * 20, 0, 0);
  const unsigned x1 = __builtin_amdgcn_raw_buffer_load_b32(c.rx, lane * 20 + 16, 0, 0);
  const unsigned epoch = __hip_atomic_load((gu32*)(a.ws + WS_EPOCH), RLX_AGENT);
  c.vq0 = 20 * g * (HD * 2) + 16 * j;
  c.vq1 = c.vq0 + (j < 8 ? 256 : 10 * HD * 2 - 128);
  c.sq = 80 * w * (HD * 2) + h * (DH * 2);
  c.rot = ROT ? (s * 5) >> 2 : 0;
  c.vk = r * (DH * 2) + cc * 16;
  c.vv = g * (DH * 4) + j * 16;
  c.skv = (wg * 512 + w * 64) * (DH * 2);
  SB();
  issue<0, LOOKAHEAD>(c);
  STAMP(1);

  const int j9 = j < 9 ? j : 9;
  c.vo = 6 * g * (D * 2) + j9 * 16;
  c.so = (DH * h + 24 * w) * (D * 2) + 160 * s;
  c.vd = 2 * (g < 2 ? g : 2) * (D * 2) + j9 * 16;
  c.sd = (48 * h + 6 * w) * (D * 2) + 160 * s;
  c.gup = (const GAS unsigned char*)(j < 8 ? a.wg : a.wu) + (unsigned)((80 * s + 10 * w + g) * (F * 2) + h * 96 + (cc < 5 ? cc : 5) * 16);
  c.g1 = (g < 2 ? 8 : 6) * (F * 2);
  c.ge = (gu64*)(a.ws + WS_G0) + (h * 8 + cc) * 128;
  c.fidx = 6 * w + (r < 5 ? r : 5);
  const int cp = 5 * (h >> 2) + (w - 3);
  const int fcol = 80 * s + 2 * cp + (lane >> 5);
  c.vx = 2 * fcol;
  unsigned mk[4];
#pragma unroll
  for (int v = 0; v < 4; ++v) mk[v] = (cc >> 1) == v ? ((cc & 1) ? 0xffff0000u : 0x0000ffffu) : 0u;

  unsigned xa[10], xg[3];
  {
    float xv[10] = {f_lo(x4[0]), f_hi(x4[0]), f_lo(x4[1]), f_hi(x4[1]), f_lo(x4[2]), f_hi(x4[2]), f_lo(x4[3]), f_hi(x4[3]), f_lo(x1), f_hi(x1)};
    float sm = 0.f;
#pragma unroll
    for (int i = 0; i < 10; ++i) sm += xv[i];
    const float mean = wave_sum(sm) * (1.0f / D);
    float sq = 0.f;
#pragma unroll
    for (int i = 0; i < 10; ++i) { const float d = xv[i] - mean; sq = fmaf(d, d, sq); }
    const float var = wave_sum(sq) * (1.0f / D);
    const float m = rbf(mean);
    const float den = rbf(__builtin_sqrtf(rbf(rbf(var) + 1e-5f)));
    const float inv = 1.0f / den;
    LAS v2u* xw = (LAS v2u*)(L + L_XX + lane * 40);
#pragma unroll
    for (int i = 0; i < 5; ++i) {
      const float n0 = rbf(xv[2 * i] - m) * inv, n1 = rbf(xv[2 * i + 1] - m) * inv;
      xw[i] = v2u{pk2(n0, n0), pk2(n1, n1)};
    }
    asm volatile("s_waitcnt lgkmcnt(0)" ::: "memory");
    const LAS unsigned* xr = (const LAS unsigned*)(L + L_XX);
#pragma unroll
    for (int t = 0; t < 10; ++t) xa[t] = xr[80 * w + 10 * r + rot10(t, c.rot)];
    xg[0] = xr[80 * s + 10 * w + g];
    xg[1] = xr[80 * s + 10 * w + 4 + g];
    xg[2] = g < 2 ? xr[80 * s + 10 * w + 8 + (g & 1)] : 0u;
  }
  const unsigned tag = __builtin_amdgcn_readfirstlane(epoch) + 1u;
  STAMP(2);

  v4f dq[3] = {v4f{0, 0, 0, 0}, v4f{0, 0, 0, 0}, v4f{0, 0, 0, 0}};
  q_step<0>(c, xa, mk, dq);
  v4f dg = {0, 0, 0, 0};
#pragma unroll
  for (int n = 0; n < 3; ++n) dg = mfma(diag(xg[n], mk), c.gu[n], dg);
  PIN1(dg);
  SB();
  issue<issued_after(pos_of(WQ, 29)), issued_after(pos_of(GU, 2))>(c);
  put192(L, L_RQ, dq, w, lane);
  if (lane < 32 && cc < 6) *(LAS v4f*)((LAS float*)(L + L_RG) + w * 96 + (j >> 3) * 48 + 8 * cc + 4 * g) = dg;
  STAMP(3);
  wg_barrier();
  if (tid < 192) {
    const LAS float* p = (const LAS float*)(L + L_RQ) + tid;
    float acc = 0.f;
#pragma unroll
    for (int i = 0; i < 16; ++i) acc += p[i * 192];
    ((LAS __bf16*)(L + L_QB))[tid] = (__bf16)acc;
  } else if (tid < 288) {
    const LAS float* p = (const LAS float*)(L + L_RG) + (tid - 192);
    float acc = 0.f;
#pragma unroll
    for (int i = 0; i < 8; ++i) acc += p[i * 96];
    __hip_atomic_store((gu64*)(a.ws + WS_G0) + wg * 128 + (tid - 192), granule(acc, tag), RLX_AGENT);
  }
  wg_barrier();
  STAMP(4);

  v4u q[3];
#pragma unroll
  for (int u = 0; u < 3; ++u) q[u] = *(const LAS v4u*)(L + L_QB + 16 * (cc + 8 * u));
  float sc[8];
  k_step<0>(c, q, sc);
  constexpr float SCALE = 0.07216878364870322f, LOG2E = 1.4426950408889634f;
  float mw = -INFINITY;
#pragma unroll
  for (int t = 0; t < 8; ++t) { sc[t] = rbf(rbf(sum8(sc[t])) * SCALE); mw = fmaxf(mw, sc[t]); }
  mw = grp_max(mw);
  {
    float lw = 0.f;
#pragma unroll
    for (int t = 0; t < 8; ++t) lw += __builtin_amdgcn_exp2f((sc[t] - mw) * LOG2E);
    lw = grp_sum(lw);
    if (lane == 0) *(LAS v2u*)(L + L_ML + 8 * w) = v2u{__float_as_uint(mw), __float_as_uint(lw)};
  }
  STAMP(5);
  wg_barrier();
  unsigned pp[8];
  {
    const v2u ml = *(const LAS v2u*)(L + L_ML + 8 * cc);
    const float mc = __uint_as_float(ml[0]);
    const float M = max8(mc);
    const float Ls = sum8(__uint_as_float(ml[1]) * __builtin_amdgcn_exp2f((mc - M) * LOG2E));
    if (tid < 2) __hip_atomic_store((gu64*)(a.ws + WS_G0) + wg * 128 + 96 + tid, granule(tid ? Ls : M, tag), RLX_AGENT);
#pragma unroll
    for (int t = 0; t < 8; ++t) { const float p = __builtin_amdgcn_exp2f((sc[t] - M) * LOG2E); pp[t] = pk2(p, p); }
  }

  v4f dv[3] = {v4f{0, 0, 0, 0}, v4f{0, 0, 0, 0}, v4f{0, 0, 0, 0}};
  v_step<0>(c, pp, mk, dv);
  put192(L, L_RO, dv, w, lane);
  STAMP(6);
  wg_barrier();
  if (tid < 192) {
    const LAS float* p = (const LAS float*)(L + L_RO) + tid;
    float acc = 0.f;
#pragma unroll
    for (int i = 0; i < 16; ++i) acc += p[i * 192];
    __hip_atomic_store((gu64*)(a.ws + WS_G1) + wg * 192 + tid, granule(acc, tag), RLX_AGENT);
  }
  STAMP(7);

  for (unsigned spin = 0; spin < SPIN_CAP; ++spin) {
    const unsigned bad = gbad(c.eg, tag) | gbad(c.eu, tag) | gbad(c.em, tag) | gbad(c.el, tag);
    if (__all(bad == 0u)) break;
    c.eg = __hip_atomic_load(c.ge + c.fidx, RLX_AGENT);
    c.eu = __hip_atomic_load(c.ge + 48 + c.fidx, RLX_AGENT);
    c.em = __hip_atomic_load(c.ge + 96, RLX_AGENT);
    c.el = __hip_atomic_load(c.ge + 97, RLX_AGENT);
  }
  float fac;
  v4f ylo = {0, 0, 0, 0}, yhi = {0, 0, 0, 0};
  {
    const float ms = gval(c.em), M = max8(ms);
    const float e = __builtin_amdgcn_exp2f((ms - M) * LOG2E);
    fac = e / sum8(e * gval(c.el));
    const float gg = rbf(sum8(gval(c.eg))), uu = rbf(sum8(gval(c.eu)));
    const float sg = rbf(1.0f / (1.0f + __builtin_amdgcn_exp2f(-gg * LOG2E)));
    const float hh = r < 6 ? sg * uu : 0.f;
    const v4u ah = diag(pk2(hh, hh), mk);
    ylo = mfma(ah, c.wo[6], ylo);
    yhi = mfma(ah, c.wo[7], yhi);
  }
  {
    gu64* go = (gu64*)(a.ws + WS_G1) + (h * 8 + cc) * 192 + 24 * w + 3 * r;
    u64 o0, o1, o2;
    for (unsigned spin = 0;; ++spin) {
      o0 = __hip_atomic_load(go, RLX_AGENT);
      o1 = __hip_atomic_load(go + 1, RLX_AGENT);
      o2 = __hip_atomic_load(go + 2, RLX_AGENT);
      const unsigned bad = gbad(o0, tag) | gbad(o1, tag) | gbad(o2, tag);
      if (__all(bad == 0u) || spin >= SPIN_CAP) break;
    }
    STAMP(8);
    const float f0 = sum8(gval(o0) * fac), f1 = sum8(gval(o1) * fac), f2 = sum8(gval(o2) * fac);
    const v4u a0 = diag(pk2(f0, f0), mk), a1 = diag(pk2(f1, f1), mk), a2 = diag(pk2(f2, f2), mk);
    ylo = mfma(a0, c.wo[0], ylo); yhi = mfma(a0, c.wo[3], yhi);
    ylo = mfma(a1, c.wo[1], ylo); yhi = mfma(a1, c.wo[4], yhi);
    ylo = mfma(a2, c.wo[2], ylo); yhi = mfma(a2, c.wo[5], yhi);
    const v4f y = (lane >> 5) ? yhi : ylo;
    if (j < 10) *(LAS v4f*)((LAS float*)(L + L_RY) + (2 * w + (lane >> 5)) * 80 + 8 * j + 4 * (g & 1)) = y;
  }
  wg_barrier();
  STAMP(9);
  if (tid < 80) {
    const LAS float* p = (const LAS float*)(L + L_RY) + tid;
    float acc = 0.f;
#pragma unroll
    for (int i = 0; i < 16; ++i) acc += p[i * 80];
    __hip_atomic_store((gu64*)(a.ws + WS_G2) + wg * 80 + tid, granule(acc, tag), RLX_AGENT);
  }
  if ((h & 3) == 0 && w >= 3) {
    gu64* gy = (gu64*)(a.ws + WS_G2) + ((lane & 31) * 8 + s) * 80 + 2 * cp + (lane >> 5);
    u64 y;
    for (unsigned spin = 0;; ++spin) {
      y = __hip_atomic_load(gy, RLX_AGENT);
      if (__all(gbad(y, tag) == 0u) || spin >= SPIN_CAP) break;
    }
    float t = sum8(gval(y));
    t += dpp<RMIR>(t);
    t = x16_sum(t);
    t += f_lo(c.xf);
    if ((lane & 31) == 0) ((GAS __bf16*)a.out)[fcol] = (__bf16)t;
    if (wg == 0 && tid == 192) __hip_atomic_store((gu32*)(a.ws + WS_EPOCH), tag, RLX_AGENT);
  }
  STAMP(10);
  {
    const u64 lim = 4000 + (PROBE >= 0 ? 4 * (st[PROBE >= 0 ? PROBE : 0] - st[0]) : 0);
    while (__builtin_amdgcn_s_memrealtime() - st[0] < lim) __builtin_amdgcn_s_sleep(1);
  }
}

}

extern "C" void kernel_launch(void* const* d_in, const int* in_sizes, int n_in, void* d_out, int out_size, void* d_ws, size_t ws_size,
                              hipStream_t stream) {
  Args a;
  a.x = (const u16*)d_in[0];
  a.k = (const u16*)d_in[1];
  a.v = (const u16*)d_in[2];
  a.wq = (const u16*)d_in[3];
  a.wo = (const u16*)d_in[4];
  a.wg = (const u16*)d_in[6];
  a.wu = (const u16*)d_in[7];
  a.wd = (const u16*)d_in[8];
  a.out = (u16*)d_out;
  a.ws = (unsigned char*)d_ws;
  fused_kernel<<<NWG, NTHR, 0, stream>>>(a);
}
```

```cpp
#include <hip/hip_runtime.h>
#include <stdint.h>

namespace {

typedef unsigned short u16;
typedef unsigned long long u64;
typedef unsigned v4u __attribute__((ext_vector_type(4)));
typedef unsigned v2u __attribute__((ext_vector_type(2)));
typedef float v4f __attribute__((ext_vector_type(4)));
typedef float v2f __attribute__((ext_vector_type(2)));
typedef __bf16 bf16x8 __attribute__((ext_vector_type(8)));
typedef __bf16 bf16x2 __attribute__((ext_vector_type(2)));
#define GAS __attribute__((address_space(1)))
#define LAS __attribute__((address_space(3)))
typedef GAS u64 gu64;
typedef GAS unsigned gu32;
#define RLX_AGENT __ATOMIC_RELAXED, __HIP_MEMORY_SCOPE_AGENT

constexpr int D = 640, H = 32, SK = 4096, DH = 192, F = 1536, HD = H * DH;
constexpr int NWG = 256, NTHR = 832;

#define WMIN 5
#define WMAX 15
#define WSH 12
#define TAU 2730
#define TOFF 144
#define LSTAR 28
#define EXTRAP 1.0f
#define Q_AT 500
#define KPRE 3
#define LOOK 1
#define WO_FIRST 192
#define WO_SPREAD 20
#define WO_HOLD 40
#define ODELAY 60
#define HV_AT 76
#define HV_STEP 3
#define LSTAR3 LSTAR
#define LSTARW LSTAR3
#define SMALL 7.62939453125e-06f
constexpr int NT = 2, SC1 = 16;

constexpr int WS_EPOCH = 0;
constexpr int WS_GS = 4096;
constexpr int WS_GT = WS_GS + H * 128;
constexpr int WS_GC = WS_GT + 512;
constexpr int WS_G1 = WS_GC + 144 * 768;
constexpr int WS_G2 = WS_G1 + NWG * 768;
constexpr int WS_ACC = WS_G2 + H * 2560;
constexpr int WS_END = WS_ACC + 80 * 128;
constexpr unsigned TAG8 = 0xA5u;
constexpr unsigned SPIN_CAP = 1u << 15;
constexpr unsigned LSPIN_CAP = 1u << 21;

constexpr int RT = 48;
constexpr int L_RING = 0;
constexpr int L_RY = L_RING + RT * 3072;
constexpr int L_R1 = L_RY;
constexpr int L_XN = L_R1 + 2304;
constexpr int L_PT = L_RY + 16 * 144 * 4;
constexpr int L_QB = L_PT + 2048;
constexpr int L_HH = L_QB + 384;
constexpr int L_FAC = L_HH + 256;
constexpr int L_ML = L_FAC + 32;
constexpr int L_PROG = L_ML + 96;
constexpr int L_WIN = L_PROG + 256;
constexpr int L_RP = L_WIN + 256;
constexpr int L_CTL = L_RP + 256;
constexpr int L_JUNK = L_CTL + 256;
constexpr int LDS_BYTES = L_JUNK + 1024;
static_assert(LDS_BYTES <= 160 * 1024 && LDS_BYTES > 80 * 1024 && L_PROG % 16 == 0, "one workgroup per CU");
constexpr int C_RC = 0, C_B1 = 16, C_B2 = 17, C_B3 = 18, C_PT = 19, C_SV = 20, C_XN = 21, C_ADJ = 22;

__device__ __forceinline__ float dot2(unsigned a, unsigned b, float c) {
  return __builtin_amdgcn_fdot2_f32_bf16(__builtin_bit_cast(bf16x2, a), __builtin_bit_cast(bf16x2, b), c, false);
}
__device__ __forceinline__ v4f mfma(v4u a, v4u b, v4f c) {
  return __builtin_amdgcn_mfma_f32_16x16x32_bf16(__builtin_bit_cast(bf16x8, a), __builtin_bit_cast(bf16x8, b), c, 0, 0, 0);
}
__device__ __forceinline__ unsigned pk2(float lo, float hi) {
  bf16x2 p = {(__bf16)lo, (__bf16)hi};
  return __builtin_bit_cast(unsigned, p);
}
__device__ __forceinline__ float rbf(float f) { return (float)(__bf16)f; }
__device__ __forceinline__ float f_lo(unsigned u) { return __builtin_bit_cast(float, u << 16); }
__device__ __forceinline__ float f_hi(unsigned u) { return __builtin_bit_cast(float, u & 0xffff0000u); }
template <int CTRL> __device__ __forceinline__ float dpp(float x) {
  return __builtin_bit_cast(float, __builtin_amdgcn_mov_dpp(__builtin_bit_cast(int, x), CTRL, 0xf, 0xf, true));
}
constexpr int XOR1 = 0xB1, XOR2 = 0x4E, HMIR = 0x141, RMIR = 0x140, ROR8 = 0x128;
__device__ __forceinline__ float sum8(float v) { v += dpp<XOR1>(v); v += dpp<XOR2>(v); v += dpp<HMIR>(v); return v; }
__device__ __forceinline__ float max8(float v) { v = fmaxf(v, dpp<XOR1>(v)); v = fmaxf(v, dpp<XOR2>(v)); v = fmaxf(v, dpp<HMIR>(v)); return v; }
__device__ __forceinline__ float wave_sum(float v) {
  v = sum8(v);
  v += dpp<RMIR>(v);
  auto x = __builtin_amdgcn_permlane16_swap(__float_as_uint(v), __float_as_uint(v), false, false);
  v = __uint_as_float(x[0]) + __uint_as_float(x[1]);
  auto y = __builtin_amdgcn_permlane32_swap(__float_as_uint(v), __float_as_uint(v), false, false);
  return __uint_as_float(y[0]) + __uint_as_float(y[1]);
}
__device__ __forceinline__ float wave_max(float v) {
  v = max8(v);
  v = fmaxf(v, dpp<RMIR>(v));
  auto x = __builtin_amdgcn_permlane16_swap(__float_as_uint(v), __float_as_uint(v), false, false);
  v = fmaxf(__uint_as_float(x[0]), __uint_as_float(x[1]));
  auto y = __builtin_amdgcn_permlane32_swap(__float_as_uint(v), __float_as_uint(v), false, false);
  return fmaxf(__uint_as_float(y[0]), __uint_as_float(y[1]));
}
__device__ __forceinline__ float fold4(v4f a) {
  auto x = __builtin_amdgcn_permlane32_swap(__float_as_uint(a[0]), __float_as_uint(a[2]), false, false);
  auto y = __builtin_amdgcn_permlane32_swap(__float_as_uint(a[1]), __float_as_uint(a[3]), false, false);
  const float b0 = __uint_as_float(x[0]) + __uint_as_float(x[1]), b1 = __uint_as_float(y[0]) + __uint_as_float(y[1]);
  auto z = __builtin_amdgcn_permlane16_swap(__float_as_uint(b0), __float_as_uint(b1), false, false);
  return __uint_as_float(z[0]) + __uint_as_float(z[1]);
}
__device__ __forceinline__ v4u diag(unsigned xx, const unsigned (&mk)[4]) { return v4u{xx & mk[0], xx & mk[1], xx & mk[2], xx & mk[3]}; }

__device__ __forceinline__ u64 granule(unsigned v, unsigned tag) { return ((u64)tag << 32) | (u64)((v & 0xffffff00u) | TAG8); }
__device__ __forceinline__ unsigned gbad(u64 g, unsigned tag) { return ((unsigned)(g >> 32) ^ tag) | (((unsigned)g ^ TAG8) & 0xffu); }
__device__ __forceinline__ float gval(u64 g) { return __uint_as_float((unsigned)g & 0xffffff00u); }
__device__ __forceinline__ v4u tag4(unsigned t) { return v4u{t, t + (t >> 10), t + (t >> 20), t + (t >> 30)}; }
__device__ __forceinline__ unsigned tword(float v, unsigned f) { return (__float_as_uint(v) & ~1023u) | (f & 1023u); }
__device__ __forceinline__ bool tbad(v4u v, v4u t) { return (((v[0] ^ t[0]) | (v[1] ^ t[1]) | (v[2] ^ t[2]) | (v[3] ^ t[3])) & 1023u) != 0u; }
__device__ __forceinline__ v4f tval(v4u v) { return __builtin_bit_cast(v4f, v & ~1023u); }

__device__ __forceinline__ unsigned clk() { return (unsigned)__builtin_amdgcn_s_memrealtime(); }
#define ANY(c) (__builtin_amdgcn_ballot_w64(c) != 0ull)
#define LGKM0() asm volatile("s_waitcnt lgkmcnt(0)" ::: "memory")
#define SB() __builtin_amdgcn_sched_barrier(0)
#define PIN(x) asm volatile("" : "+v"(x))
#define PINS(x) asm volatile("" : "+s"(x))
#define NOUNROLL _Pragma("clang loop unroll(disable)")
#define RSRC(p, n) __builtin_amdgcn_make_buffer_rsrc((void*)(p), 0, (n), 0x00020000)

template <int SLEEP> __device__ __forceinline__ int lds_wait(unsigned a, int need) {
  int v, sv, n;
  asm volatile("s_mov_b32 %[n], %c[cap]\n\t"
               "s_branch 2f\n"
               "1:\n\t"
               "s_sleep %c[sl]\n"
               "2:\n\t"
               "ds_read_b32 %[v], %[a]\n\t"
               "s_sub_u32 %[n], %[n], 1\n\t"
               "s_cbranch_scc1 3f\n\t"
               "s_waitcnt lgkmcnt(0)\n\t"
               "v_readfirstlane_b32 %[sv], %[v]\n\t"
               "s_cmp_lt_i32 %[sv], %[need]\n\t"
               "s_cbranch_scc1 1b\n"
               "3:\n\t"
               "s_waitcnt lgkmcnt(0)\n\t"
               "s_nop 1"
               : [v] "=&v"(v), [sv] "=&s"(sv), [n] "=&s"(n) : [a] "v"(a), [need] "s"(need), [sl] "i"(SLEEP), [cap] "i"(LSPIN_CAP) : "memory", "scc");
  return sv;
}
template <int MASK = 1> __device__ __forceinline__ void lds_add(unsigned a, int one) {
  asm volatile("s_waitcnt lgkmcnt(0)\n\ts_mov_b64 exec, %c2\n\tds_add_u32 %0, %1\n\ts_mov_b64 exec, -1" :: "v"(a), "v"(one), "i"(MASK) : "memory");
}
template <int SLEEP> __device__ __forceinline__ v4u again(__amdgpu_buffer_rsrc_t rs, int off, v4u t4, v4u v) {
  NOUNROLL
  for (unsigned spin = 0; spin < SPIN_CAP; ++spin) {
    __builtin_amdgcn_s_sleep(SLEEP);
    asm volatile("" ::: "memory");
    if (tbad(v, t4)) v = __builtin_amdgcn_raw_buffer_load_b128(rs, off, 0, SC1);
    if (!ANY(tbad(v, t4))) break;
  }
  return v;
}
template <int SLEEP> __device__ __forceinline__ u64 fetch(gu64* p, unsigned tag) {
  u64 v = __hip_atomic_load(p, RLX_AGENT);
  NOUNROLL
  for (unsigned spin = 0; ANY(gbad(v, tag) != 0u) && spin < SPIN_CAP; ++spin) {
    __builtin_amdgcn_s_sleep(SLEEP);
    v = __hip_atomic_load(p, RLX_AGENT);
  }
  return v;
}
__device__ __forceinline__ int col_off(int c, int q, int g, bool& live) {
  live = g <= (16 * c + 15) / 9 - (16 * c) / 9;
  return WS_GC + c * 768 + (live ? g : 0) * 256 + q * 16;
}

#define STAMP(i) do { } while (0)
#define STAMP_OUT() do { } while (0)

struct Args {
  const u16 *x, *k, *v, *wq, *wo, *wg, *wu, *wd;
  u16* out;
  unsigned char* ws;
};

__global__ void __launch_bounds__(NTHR, 4) fused_kernel(Args a) {
  __shared__ __attribute__((aligned(16))) unsigned char smem[LDS_BYTES];
  LAS unsigned char* L = (LAS unsigned char*)smem;
  const unsigned lb = (unsigned)(u64)L;
  STAMP(0);
  u64 t64 = __builtin_amdgcn_s_memrealtime();
  const int lane = threadIdx.x & 63, pw = __builtin_amdgcn_readfirstlane(threadIdx.x >> 6);
  const int b = blockIdx.x, h = (b & 7) * 4 + (b >> 6), s = (b >> 3) & 7, wg = h * 8 + s;
  if (pw == 12 && s == 0) {
    asm volatile("s_getpc_b64 s[40:41]\n"
                 "1:\n\t"
                 ".subsection 1\n2:\n\t.subsection 0\n\t"
                 "s_mov_b32 s42, 2b-1b\n\t"
                 "s_mov_b32 s43, 0x20000\n\t"
                 "s_and_b32 s41, s41, 0xffff\n\t"
                 "s_mov_b32 m0, %1\n\t"
                 "s_movk_i32 s44, 0x1000\n\ts_movk_i32 s45, 0x2000\n\ts_movk_i32 s46, 0x3000\n\ts_movk_i32 s47, 0x4000\n\t"
                 "buffer_load_dwordx4 %0, s[40:43], 0 offen lds\n\t"
                 "buffer_load_dwordx4 %0, s[40:43], s44 offen lds\n\t"
                 "buffer_load_dwordx4 %0, s[40:43], s45 offen lds\n\t"
                 "buffer_load_dwordx4 %0, s[40:43], s46 offen lds\n\t"
                 "buffer_load_dwordx4 %0, s[40:43], s47 offen lds"
                 :: "v"(lane * 16 + (b >> 6) * 1024), "s"(lb + L_JUNK) : "memory", "scc", "s40", "s41", "s42", "s43", "s44", "s45", "s46", "s47");
  }
  const int w = pw == 0 ? 10 : pw >= 11 ? pw : pw - 1;
  const int r = lane >> 3, cc = lane & 7, g = lane >> 4, j = lane & 15;
  const unsigned prog = lb + L_PROG + 4 * lane, ctl = lb + L_CTL;
  const int kvbase = wg * (512 * DH * 2);

  const int unit = 9 * b + w, ucol = unit >> 4, urow = 40 * (unit & 15);
  int ustr = ucol < 96 ? HD * 2 : F * 2;
  int uvo = r * ustr + cc * 16, uso = urow * ustr + (ucol < 96 ? ucol : ucol < 120 ? ucol - 96 : ucol - 120) * 128;
  PIN(uvo); PINS(uso); PINS(ustr);
  unsigned char* ws = a.ws;
  PINS(ws); PINS(t64);
  const unsigned t_start = (unsigned)t64;
  v4u wt[5], x4;
  unsigned x1;
  if (w < 9) {
    const auto rs = RSRC(ucol < 96 ? a.wq : ucol < 120 ? a.wg : a.wu, D * ustr);
#pragma unroll
    for (int o = 0; o < 5; ++o) wt[o] = __builtin_amdgcn_raw_buffer_load_b128(rs, uvo, uso + 8 * o * ustr, NT);
  } else if (w == 10) {
    const auto rx = RSRC(a.x, D * 2);
    x4 = __builtin_amdgcn_raw_buffer_load_b128(rx, lane * 20, 0, 0);
    x1 = __builtin_amdgcn_raw_buffer_load_b32(rx, lane * 20 + 16, 0, 0);
  }
  SB();

  if (w == 11) {
    *(LAS v4u*)(L + L_PROG + 16 * lane) = v4u{0, 0, 0, 0};
    __builtin_amdgcn_s_setprio(3);
    static_assert(RT == 48 && WMAX * 3 + 3 <= 63 && WMAX - WMIN < 12, "loader loop");
    unsigned rca = ctl + 4 * C_RC, tv0, tv1;
    unsigned so = kvbase, dst = lb + L_RING, tn = (t_start + TOFF) << 8, n = 0, adj = 0, spin = LSPIN_CAP, wl, t0, t1;
#define TILE_ISSUE \
        "s_mov_b32 m0, %[dst]\n\t" \
        "s_add_u32 %[dst], %[dst], %[c3k]\n\t" \
        "buffer_load_dwordx4 %[vo], s[40:43], %[so] offen nt lds\n\t" \
        "buffer_load_dwordx4 %[vo], s[40:43], %[so] offen offset:1024 nt lds\n\t" \
        "buffer_load_dwordx4 %[vo], s[40:43], %[so] offen offset:2048 nt lds\n\t" \
        "s_add_u32 %[so], %[so], %[c3k]\n\t"
    asm volatile(
        "s_mov_b32 s40, %[k0]\n\ts_mov_b32 s41, %[k1]\n\ts_mov_b32 s42, %[nb]\n\ts_mov_b32 s43, 0x20000\n\t"
        "s_getpc_b64 s[46:47]\n"
        "6:\n\t"
        "s_add_u32 s46, s46, 4f-6b\n\ts_addc_u32 s47, s47, 0\n\t"
        "s_waitcnt lgkmcnt(0)\n\t"
        "s_barrier\n"
        "1:\n\t"
        "s_memrealtime s[44:45]\n\t"
        "ds_read_b32 %[tv1], %[adja]\n\t"
        "ds_read_b32 %[tv0], %[rca]\n\t"
        "s_add_u32 %[t1], %[dst], 0x3000\n\t"
        "s_waitcnt lgkmcnt(0)\n\t"
        "v_readfirstlane_b32 %[adj], %[tv1]\n\t"
        "s_cmp_lt_u32 %[n], 48\n\t"
        "s_cbranch_scc1 3f\n\t"
        "v_readfirstlane_b32 %[t0], %[tv0]\n\t"
        "s_cmp_ge_i32 %[t0], 8\n\t"
        "s_cbranch_scc1 3f\n\t"
        "s_sub_u32 %[spin], %[spin], 1\n\t"
        "s_cbranch_scc0 1b\n\t"
        "s_mov_b32 %[spin], 0\n"
        "3:\n\t"
        "s_lshl_b32 %[t0], s44, 8\n\t"
        "s_sub_u32 %[t0], %[t0], %[tn]\n\t"
        "s_sub_u32 %[t0], %[t0], %[adj]\n\t"
        "s_max_i32 %[t0], %[t0], 0\n\t"
        "s_lshr_b32 %[t0], %[t0], %c[wsh]\n\t"
        "s_min_u32 %[wl], %[t0], %c[nw]\n\t"
        "s_lshl_b32 %[t0], %[wl], 6\n\t"
        "s_add_u32 s48, s46, %[t0]\n\t"
        "s_addc_u32 s49, s47, 0\n\t"
        "s_sub_u32 %[t0], %[n], %[wl]\n\t"
        "s_sub_u32 %[t0], %[t0], %c[wmin]\n\t"
        "v_mov_b32 %[tv0], %[t0]\n\t"
        "s_setpc_b64 s[48:49]\n"
        "4:\n\t"
        ".if 0 <= %c[nw]\n"
        "9:\n\t" TILE_ISSUE
        "v_add_u32 %[tv0], 1, %[tv0]\n\t"
        "s_cmp_lg_u32 %[dst], %[t1]\n\t"
        "s_waitcnt vmcnt(%c[w3] + 0)\n\t"
        "ds_write_b32 %[pa], %[tv0]\n\t"
        "s_cbranch_scc1 9b\n\t"
        "s_branch 5f\n\t"
        ".endif\n\t"
        ".if 1 <= %c[nw]\n"
        "9:\n\t" TILE_ISSUE
        "v_add_u32 %[tv0], 1, %[tv0]\n\t"
        "s_cmp_lg_u32 %[dst], %[t1]\n\t"
        "s_waitcnt vmcnt(%c[w3] + 3)\n\t"
        "ds_write_b32 %[pa], %[tv0]\n\t"
        "s_cbranch_scc1 9b\n\t"
        "s_branch 5f\n\t"
        ".endif\n\t"
        ".if 2 <= %c[nw]\n"
        "9:\n\t" TILE_ISSUE
        "v_add_u32 %[tv0], 1, %[tv0]\n\t"
        "s_cmp_lg_u32 %[dst], %[t1]\n\t"
        "s_waitcnt vmcnt(%c[w3] + 6)\n\t"
        "ds_write_b32 %[pa], %[tv0]\n\t"
        "s_cbranch_scc1 9b\n\t"
        "s_branch 5f\n\t"
        ".endif\n\t"
        ".if 3 <= %c[nw]\n"
        "9:\n\t" TILE_ISSUE
        "v_add_u32 %[tv0], 1, %[tv0]\n\t"
        "s_cmp_lg_u32 %[dst], %[t1]\n\t"
        "s_waitcnt vmcnt(%c[w3] + 9)\n\t"
        "ds_write_b32 %[pa], %[tv0]\n\t"
        "s_cbranch_scc1 9b\n\t"
        "s_branch 5f\n\t"
        ".endif\n\t"
        ".if 4 <= %c[nw]\n"
        "9:\n\t" TILE_ISSUE
        "v_add_u32 %[tv0], 1, %[tv0]\n\t"
        "s_cmp_lg_u32 %[dst], %[t1]\n\t"
        "s_waitcnt vmcnt(%c[w3] + 12)\n\t"
        "ds_write_b32 %[pa], %[tv0]\n\t"
        "s_cbranch_scc1 9b\n\t"
        "s_branch 5f\n\t"
        ".endif\n\t"
        ".if 5 <= %c[nw]\n"
        "9:\n\t" TILE_ISSUE
        "v_add_u32 %[tv0], 1, %[tv0]\n\t"
        "s_cmp_lg_u32 %[dst], %[t1]\n\t"
        "s_waitcnt vmcnt(%c[w3] + 15)\n\t"
        "ds_write_b32 %[pa], %[tv0]\n\t"
        "s_cbranch_scc1 9b\n\t"
        "s_branch 5f\n\t"
        ".endif\n\t"
        ".if 6 <= %c[nw]\n"
        "9:\n\t" TILE_ISSUE
        "v_add_u32 %[tv0], 1, %[tv0]\n\t"
        "s_cmp_lg_u32 %[dst], %[t1]\n\t"
        "s_waitcnt vmcnt(%c[w3] + 18)\n\t"
        "ds_write_b32 %[pa], %[tv0]\n\t"
        "s_cbranch_scc1 9b\n\t"
        "s_branch 5f\n\t"
        ".endif\n\t"
        ".if 7 <= %c[nw]\n"
        "9:\n\t" TILE_ISSUE
        "v_add_u32 %[tv0], 1, %[tv0]\n\t"
        "s_cmp_lg_u32 %[dst], %[t1]\n\t"
        "s_waitcnt vmcnt(%c[w3] + 21)\n\t"
        "ds_write_b32 %[pa], %[tv0]\n\t"
        "s_cbranch_scc1 9b\n\t"
        "s_branch 5f\n\t"
        ".endif\n\t"
        ".if 8 <= %c[nw]\n"
        "9:\n\t" TILE_ISSUE
        "v_add_u32 %[tv0], 1, %[tv0]\n\t"
        "s_cmp_lg_u32 %[dst], %[t1]\n\t"
        "s_waitcnt vmcnt(%c[w3] + 24)\n\t"
        "ds_write_b32 %[pa], %[tv0]\n\t"
        "s_cbranch_scc1 9b\n\t"
        "s_branch 5f\n\t"
        ".endif\n\t"
        ".if 9 <= %c[nw]\n"
        "9:\n\t" TILE_ISSUE
        "v_add_u32 %[tv0], 1, %[tv0]\n\t"
        "s_cmp_lg_u32 %[dst], %[t1]\n\t"
        "s_waitcnt vmcnt(%c[w3] + 27)\n\t"
        "ds_write_b32 %[pa], %[tv0]\n\t"
        "s_cbranch_scc1 9b\n\t"
        "s_branch 5f\n\t"
        ".endif\n\t"
        ".if 10 <= %c[nw]\n"
        "9:\n\t" TILE_ISSUE
        "v_add_u32 %[tv0], 1, %[tv0]\n\t"
        "s_cmp_lg_u32 %[dst], %[t1]\n\t"
        "s_waitcnt vmcnt(%c[w3] + 30)\n\t"
        "ds_write_b32 %[pa], %[tv0]\n\t"
        "s_cbranch_scc1 9b\n\t"
        "s_branch 5f\n\t"
        ".endif\n\t"
        ".if 11 <= %c[nw]\n"
        "9:\n\t" TILE_ISSUE
        "v_add_u32 %[tv0], 1, %[tv0]\n\t"
        "s_cmp_lg_u32 %[dst], %[t1]\n\t"
        "s_waitcnt vmcnt(%c[w3] + 33)\n\t"
        "ds_write_b32 %[pa], %[tv0]\n\t"
        "s_cbranch_scc1 9b\n\t"
        "s_branch 5f\n\t"
        ".endif\n\t"
        "5:\n\t"
        "s_add_u32 %[n], %[n], 4\n\t"
        "s_add_u32 %[tn], %[tn], %c[tau4]\n\t"
        "s_cmp_eq_u32 %[n], 0x7c\n\t"
        "s_cbranch_scc1 10f\n\t"
        "s_and_b32 %[t0], %[n], 7\n\t"
        "s_cbranch_scc1 1b\n\t"
        "s_lshl_b32 %[t0], s44, 4\n\t"
        "s_and_b32 %[t0], %[t0], 0xfffff0\n\t"
        "s_or_b32 %[t0], %[t0], %[wl]\n\t"
        "s_lshl_b32 %[t1], %[n], 24\n\t"
        "s_or_b32 %[t0], %[t0], %[t1]\n\t"
        "v_mov_b32 %[tv1], %[t0]\n\t"
        "ds_write_b32 %[pa], %[tv1] offset:%c[wof]\n\t"
        "s_cmp_gt_u32 %[n], 48\n\t"
        "s_cselect_b32 %[t0], 4, 0\n\t"
        "v_add_u32 %[rca], %[t0], %[rca]\n\t"
        "s_cmp_eq_u32 %[dst], %[rend]\n\t"
        "s_cselect_b32 %[dst], %[ring], %[dst]\n\t"
        "s_cmp_lg_u32 %[n], 64\n\t"
        "s_cbranch_scc1 1b\n\t"
        "s_mov_b32 s40, %[v0]\n\ts_mov_b32 s41, %[v1]\n\t"
        "s_sub_u32 %[so], %[so], 0x30000\n\t"
        "s_branch 1b\n"
        "10:\n\t"
        "s_sub_u32 %[t0], %c[nw], %[wl]\n\t"
        "s_lshl_b32 %[t0], %[t0], 4\n\t"
        "s_add_u32 %[t0], %[t0], 8f-4b\n\t"
        "s_add_u32 s48, s46, %[t0]\n\t"
        "s_addc_u32 s49, s47, 0\n\t"
        "s_setpc_b64 s[48:49]\n"
        "8:\n\t"
        ".if 19 < %c[wmax]\n\tv_add_u32 %[tv0], 1, %[tv0]\n\ts_waitcnt vmcnt(57)\n\tds_write_b32 %[pa], %[tv0]\n\t.endif\n\t"
        ".if 18 < %c[wmax]\n\tv_add_u32 %[tv0], 1, %[tv0]\n\ts_waitcnt vmcnt(54)\n\tds_write_b32 %[pa], %[tv0]\n\t.endif\n\t"
        ".if 17 < %c[wmax]\n\tv_add_u32 %[tv0], 1, %[tv0]\n\ts_waitcnt vmcnt(51)\n\tds_write_b32 %[pa], %[tv0]\n\t.endif\n\t"
        ".if 16 < %c[wmax]\n\tv_add_u32 %[tv0], 1, %[tv0]\n\ts_waitcnt vmcnt(48)\n\tds_write_b32 %[pa], %[tv0]\n\t.endif\n\t"
        ".if 15 < %c[wmax]\n\tv_add_u32 %[tv0], 1, %[tv0]\n\ts_waitcnt vmcnt(45)\n\tds_write_b32 %[pa], %[tv0]\n\t.endif\n\t"
        ".if 14 < %c[wmax]\n\tv_add_u32 %[tv0], 1, %[tv0]\n\ts_waitcnt vmcnt(42)\n\tds_write_b32 %[pa], %[tv0]\n\t.endif\n\t"
        ".if 13 < %c[wmax]\n\tv_add_u32 %[tv0], 1, %[tv0]\n\ts_waitcnt vmcnt(39)\n\tds_write_b32 %[pa], %[tv0]\n\t.endif\n\t"
        ".if 12 < %c[wmax]\n\tv_add_u32 %[tv0], 1, %[tv0]\n\ts_waitcnt vmcnt(36)\n\tds_write_b32 %[pa], %[tv0]\n\t.endif\n\t"
        ".if 11 < %c[wmax]\n\tv_add_u32 %[tv0], 1, %[tv0]\n\ts_waitcnt vmcnt(33)\n\tds_write_b32 %[pa], %[tv0]\n\t.endif\n\t"
        ".if 10 < %c[wmax]\n\tv_add_u32 %[tv0], 1, %[tv0]\n\ts_waitcnt vmcnt(30)\n\tds_write_b32 %[pa], %[tv0]\n\t.endif\n\t"
        ".if 9 < %c[wmax]\n\tv_add_u32 %[tv0], 1, %[tv0]\n\ts_waitcnt vmcnt(27)\n\tds_write_b32 %[pa], %[tv0]\n\t.endif\n\t"
        ".if 8 < %c[wmax]\n\tv_add_u32 %[tv0], 1, %[tv0]\n\ts_waitcnt vmcnt(24)\n\tds_write_b32 %[pa], %[tv0]\n\t.endif\n\t"
        ".if 7 < %c[wmax]\n\tv_add_u32 %[tv0], 1, %[tv0]\n\ts_waitcnt vmcnt(21)\n\tds_write_b32 %[pa], %[tv0]\n\t.endif\n\t"
        ".if 6 < %c[wmax]\n\tv_add_u32 %[tv0], 1, %[tv0]\n\ts_waitcnt vmcnt(18)\n\tds_write_b32 %[pa], %[tv0]\n\t.endif\n\t"
        ".if 5 < %c[wmax]\n\tv_add_u32 %[tv0], 1, %[tv0]\n\ts_waitcnt vmcnt(15)\n\tds_write_b32 %[pa], %[tv0]\n\t.endif\n\t"
        ".if 4 < %c[wmax]\n\tv_add_u32 %[tv0], 1, %[tv0]\n\ts_waitcnt vmcnt(12)\n\tds_write_b32 %[pa], %[tv0]\n\t.endif\n\t"
        ".if 3 < %c[wmax]\n\tv_add_u32 %[tv0], 1, %[tv0]\n\ts_waitcnt vmcnt(9)\n\tds_write_b32 %[pa], %[tv0]\n\t.endif\n\t"
        ".if 2 < %c[wmax]\n\tv_add_u32 %[tv0], 1, %[tv0]\n\ts_waitcnt vmcnt(6)\n\tds_write_b32 %[pa], %[tv0]\n\t.endif\n\t"
        ".if 1 < %c[wmax]\n\tv_add_u32 %[tv0], 1, %[tv0]\n\ts_waitcnt vmcnt(3)\n\tds_write_b32 %[pa], %[tv0]\n\t.endif\n\t"
        ".if 0 < %c[wmax]\n\tv_add_u32 %[tv0], 1, %[tv0]\n\ts_waitcnt vmcnt(0)\n\tds_write_b32 %[pa], %[tv0]\n\t.endif\n\t"
        "s_nop 0"
        : [so] "+&s"(so), [dst] "+&s"(dst), [tn] "+&s"(tn), [n] "+&s"(n), [adj] "+&s"(adj), [spin] "+&s"(spin), [rca] "+&v"(rca),
          [wl] "=&s"(wl), [t0] "=&s"(t0), [t1] "=&s"(t1), [tv0] "=&v"(tv0), [tv1] "=&v"(tv1)
        : [k0] "s"((unsigned)(u64)a.k), [k1] "s"((unsigned)((u64)a.k >> 32) & 0xffffu), [v0] "s"((unsigned)(u64)a.v),
          [v1] "s"((unsigned)((u64)a.v >> 32) & 0xffffu), [nb] "s"(H * SK * DH * 2), [ring] "s"(lb + L_RING), [rend] "s"(lb + L_RING + RT * 3072),
          [c3k] "s"(3072), [vo] "v"(lane * 16), [pa] "v"(prog), [adja] "v"(ctl + 4 * C_ADJ),
          [tau4] "i"(TAU * 4), [wsh] "i"(WSH), [nw] "i"(WMAX - WMIN), [wmin] "i"(WMIN), [w3] "i"(3 * WMIN), [wmax] "i"(WMAX), [wof] "i"(L_WIN - L_PROG)
        : "memory", "scc", "s40", "s41", "s42", "s43", "s44", "s45", "s46", "s47", "s48", "s49");
    STAMP(7);
    STAMP_OUT();
    return;
  }

  unsigned mk[4];
#pragma unroll
  for (int v = 0; v < 4; ++v) mk[v] = (cc >> 1) == v ? ((cc & 1) ? 0xffff0000u : 0x0000ffffu) : 0u;
  unsigned tag = *(const __attribute__((address_space(4))) unsigned*)(ws + WS_EPOCH);
  SB();
  __builtin_amdgcn_s_barrier();
  asm volatile("" ::: "memory");
  SB();
  tag += 1u;
  PINS(tag);
  STAMP(1);
  const v4u t4 = tag4(tag);
  const auto rws = RSRC(ws, WS_END);
  const v4f z4 = {0, 0, 0, 0};
  const unsigned win = prog + (L_WIN - L_PROG), rp = prog + (L_RP - L_PROG);
  constexpr float SCALE = 0.07216878364870322f, LOG2E = 1.4426950408889634f;

  if (w == 10) {
    float xv[10] = {f_lo(x4[0]), f_hi(x4[0]), f_lo(x4[1]), f_hi(x4[1]), f_lo(x4[2]), f_hi(x4[2]), f_lo(x4[3]), f_hi(x4[3]), f_lo(x1), f_hi(x1)};
    float sm = 0.f;
#pragma unroll
    for (int i = 0; i < 10; ++i) sm += xv[i];
    const float mean = wave_sum(sm) * (1.0f / D);
    float sq = 0.f;
#pragma unroll
    for (int i = 0; i < 10; ++i) { const float d = xv[i] - mean; sq = fmaf(d, d, sq); }
    const float var = wave_sum(sq) * (1.0f / D);
    const float m = rbf(mean);
    const float inv = __builtin_amdgcn_rcpf(rbf(__builtin_amdgcn_sqrtf(rbf(rbf(var) + 1e-5f))));
    LAS v2u* xw = (LAS v2u*)(L + L_XN + lane * 40);
#pragma unroll
    for (int i = 0; i < 5; ++i) {
      const float n0 = rbf(xv[2 * i] - m) * inv, n1 = rbf(xv[2 * i + 1] - m) * inv;
      xw[i] = v2u{pk2(n0, n0), pk2(n1, n1)};
    }
    lds_add(ctl + 4 * C_XN, 1);
  } else if (w < 9) {
    lds_wait<1>(ctl + 4 * C_XN, 1);
    const LAS unsigned* xn = (const LAS unsigned*)(L + L_XN) + urow + r;
    v4f d = z4;
#pragma unroll
    for (int o = 0; o < 5; ++o) d = mfma(diag(xn[8 * o], mk), wt[o], d);
#pragma unroll
    for (int v = 0; v < 4; ++v) {
      const unsigned u = __float_as_uint(dpp<ROR8>(d[v]));
      d[v] += __uint_as_float(__builtin_amdgcn_permlane32_swap(u, u, false, false)[1]);
    }
    *(LAS v4f*)(L + ((lane < 32 && j < 8) ? L_R1 + 256 * w + 32 * cc + 16 * g : L_JUNK + 16 * lane)) = d;
    lds_add(ctl + 4 * C_B1, 1);
  }
  STAMP(2);

  if (w == 12) {
    const unsigned tbase = (t_start + TOFF) << 12;
    float ph_last = 0.f;
    auto tell = [&](int k, int n) {
      const unsigned v = (unsigned)lds_wait<8>(win, n << 24);
      if (b < 16 && lane == 0)
        __hip_atomic_store((gu64*)(ws + WS_GT) + 16 * k + b, granule(((v >> 4) << 12) - ((v >> 24) - 4) * (TAU * 16), tag), RLX_AGENT);
    };
    auto hear = [&](int k) {
      NOUNROLL
      for (int tries = k == 3 ? 2 : 0; tries < 3; ++tries) {
        const u64 v = __hip_atomic_load((gu64*)(ws + WS_GT) + 16 * k + j, RLX_AGENT);
        if (!ANY(gbad(v, tag) != 0u)) {
          float d = sum8((float)(int)(((unsigned)v & 0xffffff00u) - tbase));
          d = (d + dpp<ROR8>(d)) * (1.f / 256.f);
          const float e = ph_last != 0.f ? (d - ph_last) * EXTRAP : 0.f;
          ph_last = d;
          *(volatile LAS int*)(L + L_CTL + 4 * C_ADJ) = (int)(d + e) - (k == 3 ? LSTAR3 : LSTAR) * 256;
          break;
        }
        __builtin_amdgcn_s_sleep(24);
      }
    };
    if (b < 8) {
      __builtin_amdgcn_raw_buffer_store_b128(v4u{0, 0, 0, 0}, rws, lane < 40 ? WS_ACC + b * 1280 + (lane >> 2) * 128 + (lane & 3) * 16 : WS_END, 0, SC1);
      asm volatile("s_waitcnt vmcnt(0)" ::: "memory");
      if (lane == 0) __hip_atomic_store((gu64*)(ws + WS_GT) + b, granule(t_start << 8, tag), RLX_AGENT);
    }
    lds_wait<8>(prog, 8);
    {
      const u64 v = fetch<32>((gu64*)(ws + WS_GT) + cc, tag);
      *(volatile LAS int*)(L + L_CTL + 4 * C_ADJ) = (int)(sum8((float)(int)(((unsigned)v & 0xffffff00u) - (t_start << 8))) * 0.125f);
    }
    tell(1, 32);
    lds_wait<8>(ctl + 4 * C_B2, 3);
    {
      const int q = 12 * h + (j < 12 ? j : 11);
      bool lg, lu;
      const int og = col_off(96 + (q >> 4), q & 15, g, lg), ou = col_off(120 + (q >> 4), q & 15, g, lu);
      v4u vg = __builtin_amdgcn_raw_buffer_load_b128(rws, og, 0, SC1), vu = __builtin_amdgcn_raw_buffer_load_b128(rws, ou, 0, SC1);
      if (ANY(tbad(vg, t4))) vg = again<16>(rws, og, t4, vg);
      if (ANY(tbad(vu, t4))) vu = again<16>(rws, ou, t4, vu);
      const float gg = rbf(fold4(lg ? tval(vg) : z4)), uu = rbf(fold4(lu ? tval(vu) : z4));
      const float hh = rbf(__builtin_amdgcn_rcpf(1.0f + __builtin_amdgcn_exp2f(-gg * LOG2E))) * uu;
      ((LAS unsigned*)(L + L_HH))[4 * j + g] = j < 12 ? pk2(hh, hh) : 0u;
    }
    tell(2, 56);
    hear(1);
    lds_wait<4>(ctl + 4 * C_B3, 8);
    {
      const v2u ml = *(const LAS v2u*)(L + L_ML + 8 * cc);
      const float mc = __uint_as_float(ml[0]), M = max8(mc);
      const float Ls = sum8(__uint_as_float(ml[1]) * __builtin_amdgcn_exp2f((mc - M) * LOG2E));
      if (lane < 2) __hip_atomic_store((gu64*)(ws + WS_GS) + h * 16 + 8 * lane + s, granule(__float_as_uint(lane ? Ls : M), tag), RLX_AGENT);
    }
    tell(3, 80);
    hear(2);
    lds_wait<8>(prog, 88);
    {
      const float v = gval(fetch<16>((gu64*)(ws + WS_GS) + h * 16 + j, tag));
      const float o = dpp<ROR8>(v), M = max8(v);
      const float e = __builtin_amdgcn_exp2f((v - M) * LOG2E);
      const float fac = e * __builtin_amdgcn_rcpf(sum8(e * o));
      if (lane < 8) ((LAS float*)(L + L_FAC))[lane] = fac;
    }
    lds_add(ctl + 4 * C_SV, 1);
    lds_wait<8>(win, 104 << 24);
    hear(3);
    return;
  }

  if (w >= 8) {
    {
      bool live;
      const int oq = col_off(3 * h + (w - 8), j, g, live);
      NOUNROLL
      while ((int)(clk() - t_start) < Q_AT) __builtin_amdgcn_s_sleep(4);
      STAMP(3);
      v4u v = __builtin_amdgcn_raw_buffer_load_b128(rws, oq, 0, SC1);
      if (ANY(tbad(v, t4))) v = again<16>(rws, oq, t4, v);
      ((LAS __bf16*)(L + L_QB))[64 * (w - 8) + 4 * j + g] = (__bf16)fold4(live ? tval(v) : z4);
      lds_add(ctl + 4 * C_B2, 1);
      STAMP(4);
    }
    v4u tv = t4;
    int oo = (lane < 32 && j < 8) ? WS_G1 + wg * 768 + (w - 8) * 256 + 32 * cc + 16 * g : WS_END;
    PIN(tv); PIN(oo);
    lds_wait<4>(ctl + 4 * C_PT, 8);
    __builtin_amdgcn_s_setprio(2);
    STAMP(5);
    unsigned va = lb + L_RING + 16 * 3072 + r * 384 + cc * 16 + (w - 8) * 128;
    unsigned pa = lb + L_PT + 4 * r;
    volatile LAS int* rpw = (volatile LAS int*)(L + (w == 8 ? L_RP : L_JUNK)) + lane;
    v4f d0 = z4, d1 = z4;
    v4u vr[4];
    unsigned pp[4];
#pragma unroll
    for (int i = 0; i < 4; ++i) pp[i] = ((const LAS unsigned*)(L + L_PT))[8 * (60 + i) + r];
    int t = 65, te = 81, spin = LSPIN_CAP, sn = 0;
    NOUNROLL
    for (int seg = 0; seg < 5; ++seg) {
      int seen, s4;
#define DIAG(A0, A1, A2, A3, P) "v_and_b32 " A0 ", " P ", %[m0]\n\tv_and_b32 " A1 ", " P ", %[m1]\n\tv_and_b32 " A2 ", " P ", %[m2]\n\tv_and_b32 " A3 ", " P ", %[m3]\n\t"
      asm volatile(
          "s_waitcnt lgkmcnt(0)\n"
          "1:\n\t"
          "s_add_i32 %[s4], %[t], 3\n\t"
          "s_cmp_lt_i32 %[s4], %[te]\n\t"
          "s_cselect_b32 %[s4], %[s4], 0x7fff\n\t"
          "s_cmp_ge_i32 %[sn], %[s4]\n\t"
          "s_cbranch_scc0 2f\n\t"
          "ds_read2_b32 v[64:65], %[pa] offset1:8\n\t"
          "ds_read2_b32 v[66:67], %[pa] offset0:16 offset1:24\n\t"
          "ds_read_b128 v[48:51], %[va]\n\t"
          "ds_read_b128 v[52:55], %[va] offset:3072\n\t"
          "ds_read_b128 v[56:59], %[va] offset:6144\n\t"
          "ds_read_b128 v[60:63], %[va] offset:9216\n\t"
          "ds_read_b32 %[seen], %[ga]\n\t"
          "s_add_i32 %[t], %[t], 4\n\t"
          "v_add_u32 %[va], 0x3000, %[va]\n\t"
          "v_add_u32 %[pa], 0x80, %[pa]\n\t"
          "s_waitcnt lgkmcnt(5)\n\t"
          DIAG("v40", "v41", "v42", "v43", "v64") DIAG("v44", "v45", "v46", "v47", "v65")
          "s_waitcnt lgkmcnt(3)\n\t"
          "v_mfma_f32_16x16x32_bf16 %[d0], v[40:43], v[48:51], %[d0]\n\t"
          "v_mfma_f32_16x16x32_bf16 %[d1], v[44:47], v[52:55], %[d1]\n\t"
          DIAG("v40", "v41", "v42", "v43", "v66") DIAG("v44", "v45", "v46", "v47", "v67")
          "s_waitcnt lgkmcnt(0)\n\t"
          "v_readfirstlane_b32 %[sn], %[seen]\n\t"
          "v_mfma_f32_16x16x32_bf16 %[d0], v[40:43], v[56:59], %[d0]\n\t"
          "v_mfma_f32_16x16x32_bf16 %[d1], v[44:47], v[60:63], %[d1]\n\t"
          "s_cmp_lt_i32 %[t], %[te]\n\t"
          "s_cbranch_scc1 1b\n\t"
          "s_branch 4f\n"
          "2:\n\t"
          "ds_read_b32 v64, %[pa]\n\t"
          "ds_read_b32 %[seen], %[ga]\n\t"
          "ds_read_b128 v[48:51], %[va]\n\t"
          "s_waitcnt lgkmcnt(1)\n\t"
          "v_readfirstlane_b32 %[sn], %[seen]\n\t"
          DIAG("v40", "v41", "v42", "v43", "v64")
          "s_cmp_ge_i32 %[sn], %[t]\n\t"
          "s_waitcnt lgkmcnt(0)\n\t"
          "s_cbranch_scc1 3f\n\t"
          "s_sub_u32 %[spin], %[spin], 1\n\t"
          "s_cbranch_scc0 2b\n\t"
          "s_mov_b32 %[spin], 0\n"
          "3:\n\t"
          "v_mfma_f32_16x16x32_bf16 %[d0], v[40:43], v[48:51], %[d0]\n\t"
          "s_add_i32 %[t], %[t], 1\n\t"
          "v_add_u32 %[va], 0xc00, %[va]\n\t"
          "v_add_u32 %[pa], 32, %[pa]\n\t"
          "s_cmp_lt_i32 %[t], %[te]\n\t"
          "s_cbranch_scc1 1b\n"
          "4:\n\t"
          "s_nop 7"
          : [d0] "+&v"(d0), [d1] "+&v"(d1), [va] "+&v"(va), [pa] "+&v"(pa), [t] "+&s"(t), [spin] "+&s"(spin), [sn] "+&s"(sn),
            [seen] "=&v"(seen), [s4] "=&s"(s4)
          : [ga] "v"(prog), [te] "s"(te), [m0] "v"(mk[0]), [m1] "v"(mk[1]), [m2] "v"(mk[2]), [m3] "v"(mk[3])
          : "memory", "scc", "v40", "v41", "v42", "v43", "v44", "v45", "v46", "v47", "v48", "v49", "v50", "v51", "v52", "v53",
            "v54", "v55", "v56", "v57", "v58", "v59", "v60", "v61", "v62", "v63", "v64", "v65", "v66", "v67");
      if (seg == 0) lds_add<3>(ctl + 4 * (C_RC + 8) + 4 * lane, 3);
      if (seg == 1) va -= RT * 3072;
      te = seg == 0 ? 97 : seg == 1 ? 124 - WMAX - LOOK : 125;
      if (seg == 2) te = 125 - WMIN - LOOK - (lds_wait<0>(win, 0) & 15);
      if (seg == 3) {
        const auto rv = RSRC(a.v, H * SK * DH * 2);
#pragma unroll
        for (int i = 0; i < 4; ++i)
          vr[i] = __builtin_amdgcn_raw_buffer_load_b128(rv, r * 384 + cc * 16 + (w - 8) * 128, kvbase + (60 + i) * 3072, NT);
        *rpw = 1;
      }
    }
    d0 += d1;
#pragma unroll
    for (int i = 0; i < 4; ++i) d0 = mfma(diag(pp[i], mk), vr[i], d0);
    STAMP(6);
    v4u o;
#pragma unroll
    for (int v = 0; v < 4; ++v) {
      const unsigned u = __float_as_uint(dpp<ROR8>(d0[v]));
      o[v] = tword(d0[v] + __uint_as_float(__builtin_amdgcn_permlane32_swap(u, u, false, false)[1]), tv[v]);
    }
    __builtin_amdgcn_raw_buffer_store_b128(o, rws, oo, 0, SC1);
    *rpw = (clk() & 0xffffffu) | 0x2000000u;
    STAMP(7);
    STAMP_OUT();
    return;
  }

  if (w == 0) {
    const int c0 = (9 * b) >> 4, split = 16 * (c0 + 1) - 9 * b, c = c0 + (lane >> 5);
    lds_wait<0>(ctl + 4 * C_B1, 9);
    v4f acc = z4;
#pragma unroll
    for (int i = 0; i < 5; ++i) {
      const int u = 2 * i + (g & 1);
      const v4f v = *(const LAS v4f*)(L + L_R1 + 256 * u + 16 * j);
      if (u < 9 && (u < split) == (lane < 32)) acc += v;
    }
    v4u o;
#pragma unroll
    for (int v = 0; v < 4; ++v) {
      auto x = __builtin_amdgcn_permlane16_swap(__float_as_uint(acc[v]), __float_as_uint(acc[v]), false, false);
      o[v] = ((__float_as_uint(__uint_as_float(x[0]) + __uint_as_float(x[1])) + 512u) & ~1023u) | (t4[v] & 1023u);
    }
    const bool on = (g & 1) == 0 && (lane < 32 || split < 9);
    __builtin_amdgcn_raw_buffer_store_b128(o, rws, on ? WS_GC + c * 768 + (b - (16 * c) / 9) * 256 + 16 * j : WS_END, 0, SC1);
  }
  STAMP(3);

  const bool heavy = (s & 3) == 3;
  const auto rw = RSRC(w < 6 ? a.wo : a.wd, w < 6 ? HD * D * 2 : F * D * 2);
  const int so = (w < 6 ? DH * h + 32 * w : 48 * h + 32 * (w - 6)) * (D * 2);
  int vo = 4 * (w == 7 ? (r & 3) : r) * (D * 2) + cc * 16;
  v4u wb[4];

  float sc = 0.f;
  {
    const LAS unsigned char* kb = L + L_RING + r * 384 + cc * 16;
    v4u kp[KPRE][3];
#pragma unroll
    for (int i = 0; i < KPRE; ++i) {
      lds_wait<16>(prog, 8 * i + w + 1);
#pragma unroll
      for (int u = 0; u < 3; ++u) kp[i][u] = *(const LAS v4u*)(kb + (8 * i + w) * 3072 + 128 * u);
#pragma unroll
      for (int u = 0; u < 3; ++u) PIN(kp[i][u]);
      lds_add(ctl + 4 * (C_RC + i), 1);
      if (heavy) {
#pragma unroll
        for (int k = (4 * i) / KPRE; k < (4 * i + 4) / KPRE; ++k) wb[k] = __builtin_amdgcn_raw_buffer_load_b128(rw, vo + k * (D * 2), so + 128 * (8 + (s >> 2)), NT);
      }
    }
    lds_wait<2>(ctl + 4 * C_B2, 3);
    STAMP(4);
    v4u q[3];
#pragma unroll
    for (int u = 0; u < 3; ++u) q[u] = *(const LAS v4u*)(L + L_QB + 16 * (cc + 8 * u));
    auto score = [&](v4u k0, v4u k1, v4u k2) {
      float a0 = 0.f, a1 = 0.f, a2 = 0.f;
#pragma unroll
      for (int e = 0; e < 4; ++e) { a0 = dot2(k0[e], q[0][e], a0); a1 = dot2(k1[e], q[1][e], a1); a2 = dot2(k2[e], q[2][e], a2); }
      return sum8(a0 + a1 + a2);
    };
    int seen = 0, slot = 8 * KPRE + w;
    NOUNROLL
    for (int i = KPRE; i < 8; ++i) {
      if (seen < 8 * i + w + 1) seen = lds_wait<1>(prog, 8 * i + w + 1);
      v4u k0 = *(const LAS v4u*)(kb + slot * 3072), k1 = *(const LAS v4u*)(kb + slot * 3072 + 128), k2 = *(const LAS v4u*)(kb + slot * 3072 + 256);
      PIN(k0); PIN(k1); PIN(k2);
      lds_add(ctl + 4 * (C_RC + i), 1);
      const float t = score(k0, k1, k2);
      sc = cc == i ? t : sc;
      slot = slot + 8 >= RT ? slot + 8 - RT : slot + 8;
      if (i == 5) {
#pragma unroll
        for (int ii = 0; ii < KPRE; ++ii) {
          const float tt = score(kp[ii][0], kp[ii][1], kp[ii][2]);
          sc = cc == ii ? tt : sc;
        }
      }
    }
  }
  sc = rbf(rbf(sc) * SCALE);
  {
    const float mw = wave_max(sc);
    const float lw = wave_sum(__builtin_amdgcn_exp2f((sc - mw) * LOG2E));
    *(LAS v2u*)(L + (lane == 0 ? L_ML + 8 * w : L_JUNK + 8 * lane)) = v2u{__float_as_uint(mw), __float_as_uint(lw)};
    lds_add(ctl + 4 * C_B3, 1);
    lds_wait<0>(ctl + 4 * C_B3, 8);
    const float M = max8(*(const LAS float*)(L + L_ML + 8 * cc));
    const float p = __builtin_amdgcn_exp2f((sc - M) * LOG2E);
    ((LAS unsigned*)(L + L_PT))[8 * (8 * cc + w) + r] = pk2(p, p);
    lds_add(ctl + 4 * C_PT, 1);
  }
  STAMP(5);

  const int col = 64 * ((w & 2) ? 8 + (s >> 2) : s) + 32 * (w & 1) + 2 * (lane >> 2) + (lane & 1);
  float xr = f_lo(__builtin_amdgcn_raw_buffer_load_b16(RSRC(a.x, D * 2), 2 * col, 0, 0));
  int gin = WS_G1 + (h * 8 + r) * 768 + 128 * w + 16 * cc;
  int gout = cc == 0 ? WS_G2 + h * 2560 + 4 * col : WS_END;
  gu64* gacc = (gu64*)(ws + WS_ACC) + (col >> 3) * 16 + (col & 7);
  unsigned ry_w = (j >> 3) == (lane >> 5) ? lb + L_RY + ((2 * w + (lane >> 5)) * 144 + 8 * cc + 4 * (g & 1)) * 4 : lb + L_JUNK + 16 * j;
  unsigned ry_r = lb + L_RY + ((lane & 3) * 144 + 32 * w + 2 * (lane >> 2)) * 4;
  v4u tv = t4, wa[4];
  int ba = 4 * r;
  const auto rout = RSRC(a.out, D * 2);
  PIN(gin); PIN(gout); PIN(gacc); PIN(ry_w); PIN(ry_r); PIN(tv); PIN(ba); PIN(vo); PIN(xr);
  {
    const unsigned own = ((unsigned)lds_wait<8>(win, 120 << 24) >> 4) << 12;
    const int dm = (int)(((t_start + TOFF + LSTARW) << 12) + ((116 * TAU + lds_wait<0>(ctl + 4 * C_ADJ, (int)0x80000000)) << 4) - own);
    const unsigned tw = own + (dm > WO_HOLD * 4096 ? WO_HOLD * 4096 : dm) + (WO_FIRST + (7 - w) * WO_SPREAD) * TAU;
    NOUNROLL
    while ((int)((clk() << 12) - tw) < 0) __builtin_amdgcn_s_sleep(1);
    lds_wait<1>(rp, 1);
#pragma unroll
    for (int k = 0; k < 4; ++k) wa[k] = __builtin_amdgcn_raw_buffer_load_b128(rw, vo + k * (D * 2), so + 128 * s, NT);
    SB();
  }
  STAMP(6);
  lds_wait<2>(ctl + 4 * C_SV, 1);
  {
    v4u sc4;
    if (w < 6) {
      float fac = ((const LAS float*)(L + L_FAC))[r];
      const unsigned tp = (unsigned)lds_wait<2>(rp, 0x2000000);
      PIN(fac);
      NOUNROLL
      while ((int)((clk() - tp - ODELAY) << 8) < 0) __builtin_amdgcn_s_sleep(1);
      STAMP(7);
      v4u ov = __builtin_amdgcn_raw_buffer_load_b128(rws, gin, 0, SC1);
      if (__builtin_expect(ANY(tbad(ov, t4)), 0)) ov = again<2>(rws, gin, t4, ov);
      STAMP(8);
      float f = fold4(tval(ov) * fac);
      f += dpp<ROR8>(f);
      const int pf = pk2(f, f);
#pragma unroll
      for (int k = 0; k < 4; ++k) sc4[k] = __builtin_amdgcn_ds_bpermute(ba + 64 * k, pf);
    } else {
      sc4 = *(const LAS v4u*)(L + L_HH + 128 * (w - 6) + 16 * r);
    }
    const v4u a0 = diag(sc4[0], mk), a1 = diag(sc4[1], mk), a2 = diag(sc4[2], mk), a3 = diag(sc4[3], mk);
    *(LAS v4f*)(u64)ry_w = mfma(a3, wa[3], mfma(a2, wa[2], mfma(a1, wa[1], mfma(a0, wa[0], z4))));
    if (heavy) *(LAS v4f*)(u64)(ry_w + 256) = mfma(a3, wb[3], mfma(a2, wb[2], mfma(a1, wb[1], mfma(a0, wb[0], z4))));
  }
  LGKM0();
  __builtin_amdgcn_s_barrier();
  asm volatile("" ::: "memory");
  STAMP(9);
  if (w < (heavy ? 4 : 2)) {
    const LAS v2f* p = (const LAS v2f*)(u64)ry_r;
    const v2f acc = (p[0] + p[4 * 72]) + (p[8 * 72] + p[12 * 72]);
    float c0 = acc[0], c1 = acc[1];
    c0 += dpp<XOR1>(c0); c1 += dpp<XOR1>(c1);
    c0 += dpp<XOR2>(c0); c1 += dpp<XOR2>(c1);
    const float y = (lane & 1) ? c1 : c0;
    constexpr double MAGIC = 6755399441055744.0;
    const u64 add = (__builtin_bit_cast(u64, __builtin_fma((double)y, 4294967296.0, MAGIC)) << 14) | (__builtin_fabsf(y) < 4096.f ? 1u : 129u);
    u64 tot = 0;
    if ((lane & 2) == 0) tot = __hip_atomic_fetch_add(gacc, add, RLX_AGENT);
    STAMP(11);
    __builtin_amdgcn_raw_buffer_store_b128(v4u{tword(c0, tv[0]), tword(c1, tv[1]), tword(dpp<HMIR>(c0), tv[2]), tword(dpp<HMIR>(c1), tv[3])}, rws, gout, 0, SC1);
    SB();
    tot += add;
    const bool last = ((unsigned)tot & 127u) == 32u;
    float res = fmaf((float)(__builtin_bit_cast(double, (u64)((long long)tot >> 14) + 0x4338000000000000ull) - MAGIC), 2.3283064365386963e-10f, xr);
    STAMP(12);
    u64 todo = __builtin_amdgcn_ballot_w64(last && (((unsigned)tot & 0x3f80u) != 0u || !(__builtin_fabsf(res) >= SMALL)));
    if (__builtin_expect(todo != 0ull, 0)) {
      NOUNROLL
      while (todo != 0ull) {
        const int l = __builtin_ctzll(todo), c = __builtin_amdgcn_readlane(col, l);
        todo &= todo - 1;
        const v4f qf = tval(again<0>(rws, WS_G2 + (lane & 31) * 2560 + (c & ~3) * 4, t4, ~t4));
        const float e = (c & 2) ? ((c & 1) ? qf[3] : qf[2]) : ((c & 1) ? qf[1] : qf[0]);
        const float t = wave_sum(lane < 32 ? e : 0.f);
        if (lane == l) res = t + xr;
      }
    }
    __builtin_amdgcn_raw_buffer_store_b16(__builtin_bit_cast(unsigned short, (__bf16)res), rout, last ? 2 * col : D * 2, 0, 0);
    __builtin_amdgcn_raw_buffer_store_b32(tag, rws, (last && col == 0) ? WS_EPOCH : WS_END, 0, SC1);
  }
  STAMP(10);
  STAMP_OUT();
}

}

extern "C" void kernel_launch(void* const* d_in, const int* in_sizes, int n_in, void* d_out, int out_size, void* d_ws, size_t ws_size,
                              hipStream_t stream) {
  Args a;
  a.x = (const u16*)d_in[0];
  a.k = (const u16*)d_in[1];
  a.v = (const u16*)d_in[2];
  a.wq = (const u16*)d_in[3];
  a.wo = (const u16*)d_in[4];
  a.wg = (const u16*)d_in[6];
  a.wu = (const u16*)d_in[7];
  a.wd = (const u16*)d_in[8];
  a.out = (u16*)d_out;
  a.ws = (unsigned char*)d_ws;
  fused_kernel<<<NWG, NTHR, 0, stream>>>(a);
}
```
